```python
import torch

def _softmax(x, axis=-1):
    return torch.softmax(x, dim=axis)

def _gelu(x):
    return torch.nn.functional.gelu(x, approximate='tanh')

def _silu(x):
    return torch.nn.functional.silu(x)

def _sigmoid(x):
    return torch.sigmoid(x)

def _layernorm(x, axis=-1, eps=1e-05):
    m = x.mean(dim=axis, keepdim=True)
    v = x.var(dim=axis, keepdim=True, unbiased=False)
    return (x - m) / torch.sqrt(v + eps)

def _scatter_add(tgt, idx, src, axis=0):
    return tgt.clone().index_add_(axis, idx, src)

def _segment_sum(src, seg, n):
    out = torch.zeros((n,) + src.shape[1:], dtype=src.dtype)
    return out.index_add_(0, seg, src)

def _pad(x, pw):
    flat = tuple((v for pair in reversed(pw) for v in pair))
    return torch.nn.functional.pad(x, flat)
kernel_name = 'adaLN_cond__sliding_block_cached__mixer_channel_mlp__decode'

def setup_inputs():

    _w = lambda a: (lambda t: t.to(torch.bfloat16) if t.is_floating_point() else t)(torch.as_tensor(a))

    def _A_setup():
        B, Sq, D = (2, 1, 1024)
        Hq, Hkv, Sk, Dh = (32, 4, 32768, 128)
        assert Hq % Hkv == 0, (Hq, Hkv)
        return {'x': _w((lambda *s: torch.randn(*s, dtype=torch.bfloat16) * 0.05)(B, Sq, D)), 'k_cache': _w((lambda *s: torch.randn(*s, dtype=torch.bfloat16) * 3.2)(B, Hkv, Sk, Dh)), 'v_cache': _w((lambda *s: torch.randn(*s, dtype=torch.bfloat16) * 0.4)(B, Hkv, Sk, Dh)), 'wq': _w((lambda *s: torch.randn(*s, dtype=torch.bfloat16) * 3.2)(D, Hq * Dh) * D ** (-0.5)), 'wo': _w((lambda *s: torch.randn(*s, dtype=torch.bfloat16) * 0.4)(Hq * Dh, D) * (Hq * Dh) ** (-0.5))}
    _A_in = _A_setup()

    def _B_setup():
        B, S, D, F = (2, 1, 1024, 3584)
        return {'x': _w((lambda *s: torch.randn(*s, dtype=torch.bfloat16) * 0.05)(B, S, D)), 'w1': _w((lambda *s: torch.randn(*s, dtype=torch.bfloat16) * 3.2)(D, F) * D ** (-0.5)), 'b1': _w((lambda *s: torch.randn(*s, dtype=torch.bfloat16) * 3.2)(F)), 'w2': _w((lambda *s: torch.randn(*s, dtype=torch.bfloat16) * 0.2)(F, D) * F ** (-0.5)), 'b2': _w((lambda *s: torch.randn(*s, dtype=torch.bfloat16) * 0.4)(D))}
    _B_in = _B_setup()
    return {'A_x': _A_in['x'], 'A_k_cache': _A_in['k_cache'], 'A_v_cache': _A_in['v_cache'], 'A_wq': _A_in['wq'], 'A_wo': _A_in['wo'], 'B_x': _B_in['x'], 'B_w1': _B_in['w1'], 'B_b1': _B_in['b1'], 'B_w2': _B_in['w2'], 'B_b2': _B_in['b2'], 'c': _w((lambda *s: torch.randn(*s, dtype=torch.bfloat16) * 0.2)(2, 1024) * 8.0), 'w_mod': _w((lambda *s: torch.randn(*s, dtype=torch.bfloat16) * 0.2)(1024, 6 * 1024) * 1024 ** (-0.5) * 8.0)}

def reference(A_x, A_k_cache, A_v_cache, A_wq, A_wo, B_x, B_w1, B_b1, B_w2, B_b2, c, w_mod):

    def _A_ref(x, k_cache, v_cache, wq, wo):
        B, Sq, D = x.shape
        Hq, Dh = (32, 128)
        Hkv, Sk = (k_cache.shape[1], k_cache.shape[2])
        W = min(4096, max(256, Sk // 4))
        rep = Hq // Hkv
        q = (lambda x, *d: x.permute(*d))((x @ wq).reshape(B, Sq, Hq, Dh), 0, 2, 1, 3)
        k = (lambda x, n, axis: torch.repeat_interleave(x, n, dim=axis))(k_cache[:, :, Sk - W:, :], rep, axis=1)
        v = (lambda x, n, axis: torch.repeat_interleave(x, n, dim=axis))(v_cache[:, :, Sk - W:, :], rep, axis=1)
        scores = torch.einsum('bhqd,bhkd->bhqk', q, k) * Dh ** (-0.5)
        p = _softmax(scores, axis=-1)
        o = torch.einsum('bhqk,bhkd->bhqd', p, v)
        return (lambda x, *d: x.permute(*d))(o, 0, 2, 1, 3).reshape(B, Sq, Hq * Dh) @ wo

    def _B_ref(x, w1, b1, w2, b2):
        y = _layernorm(x, axis=-1)
        h = _gelu(torch.einsum('bsd,df->bsf', y, w1) + b1)
        return torch.einsum('bsf,fd->bsd', h, w2) + b2
    x = A_x
    mod = (_silu(c) @ w_mod).reshape(c.shape[0], 6, 1, -1)
    sa, ca, ga, sb, cb, gb = (mod[:, 0].clone(), mod[:, 1].clone(), mod[:, 2].clone(), mod[:, 3].clone(), mod[:, 4].clone(), mod[:, 5].clone())
    h = _layernorm(x, axis=-1) * (1 + ca) + sa
    x = x + ga * _A_ref(h, A_k_cache, A_v_cache, A_wq, A_wo)
    h = _layernorm(x, axis=-1) * (1 + cb) + sb
    x = x + gb * _B_ref(h, B_w1, B_b1, B_w2, B_b2)
    return x
```

```cpp
#include <hip/hip_runtime.h>
#include <hip/hip_bf16.h>
#include <stdint.h>

#define MIMIC 0

typedef unsigned short u16;
__device__ __forceinline__ float bf2f(u16 b) { return __uint_as_float(((unsigned)b) << 16); }
__device__ __forceinline__ u16 f2bf(float f) { unsigned u = __float_as_uint(f); u += 0x7FFFu + ((u >> 16) & 1u); return (u16)(u >> 16); }
__device__ __forceinline__ float rb(float f) {
  return f;
}
__device__ __forceinline__ float rbf(float f) { return bf2f(f2bf(f)); }

constexpr int D = 1024, HQ = 32, HKV = 4, SK = 32768, DH = 128, W = 4096, F = 3584;

__device__ float block_sum(float v, float* red) {
  for (int o = 32; o >= 1; o >>= 1) v += __shfl_xor(v, o);
  int w = threadIdx.x >> 6, l = threadIdx.x & 63;
  __syncthreads();
  if (l == 0) red[w] = v;
  __syncthreads();
  float s = 0.f;
  for (int i = 0; i < (int)(blockDim.x >> 6); ++i) s += red[i];
  return s;
}
__device__ float block_max(float v, float* red) {
  for (int o = 32; o >= 1; o >>= 1) v = fmaxf(v, __shfl_xor(v, o));
  int w = threadIdx.x >> 6, l = threadIdx.x & 63;
  __syncthreads();
  if (l == 0) red[w] = v;
  __syncthreads();
  float s = -INFINITY;
  for (int i = 0; i < (int)(blockDim.x >> 6); ++i) s = fmaxf(s, red[i]);
  return s;
}

__global__ void k_gemv(const float* in, const u16* Wt, float* out, int K, int N) {
  int n = blockIdx.x * blockDim.x + threadIdx.x;
  if (n >= N) return;
  float a0 = 0.f, a1 = 0.f;
  for (int k = 0; k < K; ++k) { float w = bf2f(Wt[(size_t)k * N + n]); a0 = fmaf(in[k], w, a0); a1 = fmaf(in[K + k], w, a1); }
  out[n] = rb(a0); out[N + n] = rb(a1);
}
__global__ void k_silu(const u16* c, float* out) {
  int i = blockIdx.x * blockDim.x + threadIdx.x;
  float x = bf2f(c[i]);
  out[i] = rb(x / (1.f + expf(-x)));
}
__device__ void ln_row(float* row, float* red) {
  float s = 0.f;
  for (int i = threadIdx.x; i < D; i += blockDim.x) s += row[i];
  float mean = block_sum(s, red) / D;
  float v = 0.f;
  for (int i = threadIdx.x; i < D; i += blockDim.x) { float d = row[i] - mean; v += d * d; }
  float var = block_sum(v, red) / D;
  float m = rb(mean), vv = rb(var);
  float den = rb(sqrtf(rb(vv + 1e-5f)));
  for (int i = threadIdx.x; i < D; i += blockDim.x) row[i] = rb(rb(row[i] - m) / den);
  __syncthreads();
}
__global__ void k_adaln(const u16* xb, const float* xf, const float* mod, int si, int ci, int second_ln, float* out) {
  __shared__ float row[D]; __shared__ float red[16];
  int b = blockIdx.x;
  for (int i = threadIdx.x; i < D; i += blockDim.x) row[i] = xb ? bf2f(xb[b * D + i]) : xf[b * D + i];
  __syncthreads();
  ln_row(row, red);
  for (int i = threadIdx.x; i < D; i += blockDim.x) {
    float ca = mod[b * 6 * D + ci * D + i], sa = mod[b * 6 * D + si * D + i];
    row[i] = rb(rb(row[i] * rb(1.f + ca)) + sa);
  }
  __syncthreads();
  if (second_ln) ln_row(row, red);
  for (int i = threadIdx.x; i < D; i += blockDim.x) out[b * D + i] = row[i];
}
__global__ void k_attn(const float* q, const u16* kc, const u16* vc, float* o) {
  __shared__ float sc[W]; __shared__ float red[16]; __shared__ float qs[DH];
  int bh = blockIdx.x, b = bh / HQ, h = bh % HQ, hk = h / (HQ / HKV);
  if (threadIdx.x < DH) qs[threadIdx.x] = q[b * HQ * DH + h * DH + threadIdx.x];
  __syncthreads();
  const u16* kb = kc + ((size_t)(b * HKV + hk) * SK + (SK - W)) * DH;
  const u16* vb = vc + ((size_t)(b * HKV + hk) * SK + (SK - W)) * DH;
  float mx = -INFINITY;
  for (int k = threadIdx.x; k < W; k += blockDim.x) {
    float s = 0.f;
    for (int d = 0; d < DH; ++d) s = fmaf(qs[d], bf2f(kb[(size_t)k * DH + d]), s);
    s = rb(rb(s) * 0.08838834764831845f);
    sc[k] = s; mx = fmaxf(mx, s);
  }
  mx = block_max(mx, red);
  float sum = 0.f;
  for (int k = threadIdx.x; k < W; k += blockDim.x) { float e = expf(sc[k] - mx); sc[k] = e; sum += e; }
  sum = block_sum(sum, red);
  for (int k = threadIdx.x; k < W; k += blockDim.x) sc[k] = rb(sc[k] / sum);
  __syncthreads();
  if (threadIdx.x < DH) {
    float a = 0.f;
    for (int k = 0; k < W; ++k) a = fmaf(sc[k], bf2f(vb[(size_t)k * DH + threadIdx.x]), a);
    o[b * HQ * DH + h * DH + threadIdx.x] = rb(a);
  }
}
__global__ void k_res1(const u16* x, const float* mod, const float* att, float* x1) {
  int i = blockIdx.x * blockDim.x + threadIdx.x; int b = i / D, d = i % D;
  x1[i] = rb(bf2f(x[i]) + rb(mod[b * 6 * D + 2 * D + d] * att[i]));
}
__global__ void k_gelu(float* h, const u16* b1) {
  int i = blockIdx.x * blockDim.x + threadIdx.x; int f = i % F;
  float x = rb(h[i] + bf2f(b1[f]));
  float t = tanhf(0.7978845608028654f * (x + 0.044715f * x * x * x));
  h[i] = rb(0.5f * x * (1.f + t));
}
__global__ void k_out(const float* x1, const float* mod, const float* m2, const u16* b2, u16* out) {
  int i = blockIdx.x * blockDim.x + threadIdx.x; int b = i / D, d = i % D;
  float y = rb(m2[i] + bf2f(b2[d]));
  out[i] = f2bf(x1[i] + rb(mod[b * 6 * D + 5 * D + d] * y));
}

extern "C" void kernel_launch(void* const* d_in, const int* in_sizes, int n_in, void* d_out, int out_size, void* d_ws, size_t ws_size, hipStream_t stream) {
  const u16* A_x = (const u16*)d_in[0]; const u16* kc = (const u16*)d_in[1]; const u16* vc = (const u16*)d_in[2];
  const u16* wq = (const u16*)d_in[3]; const u16* wo = (const u16*)d_in[4];
  const u16* w1 = (const u16*)d_in[6]; const u16* b1 = (const u16*)d_in[7]; const u16* w2 = (const u16*)d_in[8]; const u16* b2 = (const u16*)d_in[9];
  const u16* c = (const u16*)d_in[10]; const u16* wm = (const u16*)d_in[11];
  float* ws = (float*)d_ws;
  float* sc = ws; float* mod = ws + 2048; float* h = mod + 2 * 6144; float* q = h + 2048; float* o = q + 8192; float* att = o + 8192;
  float* x1 = att + 2048; float* y = x1 + 2048; float* h1 = y + 2048; float* m2 = h1 + 2 * F;
  k_silu<<<8, 256, 0, stream>>>(c, sc);
  k_gemv<<<6144 / 64, 64, 0, stream>>>(sc, wm, mod, 1024, 6144);
  k_adaln<<<2, 256, 0, stream>>>(A_x, nullptr, mod, 0, 1, 0, h);
  k_gemv<<<4096 / 64, 64, 0, stream>>>(h, wq, q, 1024, 4096);
  k_attn<<<64, 256, 0, stream>>>(q, kc, vc, o);
  k_gemv<<<1024 / 64, 64, 0, stream>>>(o, wo, att, 4096, 1024);
  k_res1<<<8, 256, 0, stream>>>(A_x, mod, att, x1);
  k_adaln<<<2, 256, 0, stream>>>(nullptr, x1, mod, 3, 4, 1, y);
  k_gemv<<<F / 64, 64, 0, stream>>>(y, w1, h1, 1024, F);
  k_gelu<<<2 * F / 256, 256, 0, stream>>>(h1, b1);
  k_gemv<<<1024 / 64, 64, 0, stream>>>(h1, w2, m2, F, 1024);
  k_out<<<8, 256, 0, stream>>>(x1, mod, m2, b2, (u16*)d_out);
}
```

```cpp
#include <hip/hip_runtime.h>
#include <stdint.h>
#pragma clang fp contract(off)

namespace mk {
#define GAS __attribute__((address_space(1)))
#define LAS __attribute__((address_space(3)))
typedef unsigned short u16;
typedef unsigned u32;
typedef unsigned long long u64;
typedef u32 v4u __attribute__((ext_vector_type(4)));
typedef u32 v2u __attribute__((ext_vector_type(2)));
typedef float f4 __attribute__((ext_vector_type(4)));
typedef float f2 __attribute__((ext_vector_type(2)));
typedef short s4 __attribute__((ext_vector_type(4)));
typedef __bf16 bf8 __attribute__((ext_vector_type(8)));
typedef __bf16 bf2 __attribute__((ext_vector_type(2)));
typedef LAS unsigned char* lds_t;

constexpr int NWG = 256, NTHR = 576;
constexpr u32 EMPTY = 0xFFFFFFFFu;
constexpr int RSRC_W3 = 0x00020000;
constexpr int SC1 = 16;
constexpr u32 SPIN_G = 1u << 17;
constexpr u32 SPIN_L = 1u << 22;
constexpr float LOG2E = 1.4426950408889634f;

constexpr int WS_EPOCH = 0, WS_READY = 256;
constexpr int WS_P1 = 4096;
constexpr int WS_P2 = WS_P1 + 128 * 512;
constexpr int WS_O3 = WS_P2 + 256 * 512;
constexpr int WS_ML = WS_O3 + 128 * 2048;
constexpr int WS_P4 = WS_ML + 128 * 128;
constexpr int WS_PG = WS_P4 + 64 * 32 * 128;
constexpr int WS_X1 = WS_PG + 4 * 64 * 8 * 128;
constexpr int WS_SC = WS_X1 + 64 * 128;
constexpr int WS_P6 = WS_SC + 64 * 128;
constexpr int WS_P7 = WS_P6 + 56 * 2048;
constexpr int WS_END = WS_P7 + 64 * 28 * 128;
constexpr int WS_STAMP = 4 << 20;

constexpr int L_CTL = 131072;
constexpr int L_STAMP = L_CTL + 256;
constexpr int L_CF = L_STAMP + 1280;
constexpr int CF_P = 2048 + 16;
constexpr int L_XF = L_CF + 2 * CF_P;
constexpr int XF_P = 1024 + 16;
constexpr int L_G = L_XF + 6 * XF_P;
constexpr int QF_P = 256 + 16;
constexpr int L_QF = L_G;
constexpr int L_PF = L_QF + 8 * QF_P;
constexpr int L_MW = L_PF + 8192;
constexpr int L_OT = L_MW + 1024;
constexpr int L_RED = L_OT + 8 * QF_P;
constexpr int L_X1 = L_RED + 256;
constexpr int L_XEND = L_X1 + 4096;
constexpr int L_GT = L_G;
constexpr int L_YEND = L_GT + 16384;
constexpr int LDS_BYTES = 160 * 1024;
static_assert(L_XEND <= LDS_BYTES && L_YEND <= LDS_BYTES, "lds");
static_assert((L_CF % 16) == 0 && (L_XF % 16) == 0 && (L_G % 16) == 0 && (L_PF % 16) == 0 && (L_MW % 16) == 0 && (L_OT % 16) == 0 && (L_RED % 16) == 0 && (L_X1 % 16) == 0, "align");
enum { C_FULL = 0  , C_FLG = 8  , C_GATE = 16  , C_CLR = 24, C_RDY = 25, C_DONE = 26 };
#define MK_NINF 16
constexpr int NINF = MK_NINF;

struct Params {
  const u16 *x, *kc, *vc, *wq, *wo, *w1, *b1, *w2, *b2, *c, *wm;
  u16* out;
  unsigned char* ws;
};

#define PIN(x) asm volatile("" : "+v"(x))
#define SCHED() __builtin_amdgcn_sched_barrier(0)
__device__ __forceinline__ float asf(u32 b) { return __builtin_bit_cast(float, b); }
__device__ __forceinline__ u32 asu(float f) { return __builtin_bit_cast(u32, f); }
__device__ __forceinline__ float bflo(u32 w) { return asf(w << 16); }
__device__ __forceinline__ float bfhi(u32 w) { return asf(w & 0xffff0000u); }
__device__ __forceinline__ u32 pk2(float lo, float hi) { bf2 v = {(__bf16)lo, (__bf16)hi}; return __builtin_bit_cast(u32, v); }
__device__ __forceinline__ float rbf(float f) { return bflo(pk2(f, f)); }
__device__ __forceinline__ u32 umin(u32 a, u32 b) { return a < b ? a : b; }
__device__ __forceinline__ u32 umax(u32 a, u32 b) { return a > b ? a : b; }
__device__ __forceinline__ u32 sane(u32 w) { return umin(w, 0xFFFEFFFFu); }
__device__ __forceinline__ u32 fsane(float f) { return umin(asu(f), 0xFFFFFFFEu); }
template <int CTRL> __device__ __forceinline__ float dpp(float x) { return __builtin_bit_cast(float, __builtin_amdgcn_mov_dpp(__builtin_bit_cast(int, x), CTRL, 0xf, 0xf, true)); }
constexpr int XOR1 = 0xB1, XOR2 = 0x4E, XOR7 = 0x141, ROR8 = 0x128;
__device__ __forceinline__ float sw32(float a, float b) { auto s = __builtin_amdgcn_permlane32_swap(asu(a), asu(b), false, false); return asf((u32)s[0]) + asf((u32)s[1]); }
__device__ __forceinline__ float sw16(float a, float b) { auto s = __builtin_amdgcn_permlane16_swap(asu(a), asu(b), false, false); return asf((u32)s[0]) + asf((u32)s[1]); }
__device__ __forceinline__ float mx32(float a) { auto s = __builtin_amdgcn_permlane32_swap(asu(a), asu(a), false, false); return fmaxf(asf((u32)s[0]), asf((u32)s[1])); }
__device__ __forceinline__ float mx16(float a) { auto s = __builtin_amdgcn_permlane16_swap(asu(a), asu(a), false, false); return fmaxf(asf((u32)s[0]), asf((u32)s[1])); }
__device__ __forceinline__ float rows4(f4 v) { return sw16(sw32(v.x, v.z), sw32(v.y, v.w)); }
__device__ __forceinline__ float row_sum16(float x) { x += dpp<XOR1>(x); x += dpp<XOR2>(x); x += dpp<XOR7>(x); x += dpp<ROR8>(x); return x; }
__device__ __forceinline__ f4 mfma(v4u a, v4u b, f4 c) { return __builtin_amdgcn_mfma_f32_16x16x32_bf16(__builtin_bit_cast(bf8, a), __builtin_bit_cast(bf8, b), c, 0, 0, 0); }
__device__ __forceinline__ v4u tr2(lds_t a0, lds_t a1) {
  s4 r0 = __builtin_amdgcn_ds_read_tr16_b64_v4i16((LAS s4*)a0), r1 = __builtin_amdgcn_ds_read_tr16_b64_v4i16((LAS s4*)a1);
  return __builtin_bit_cast(v4u, __builtin_shufflevector(r0, r1, 0, 1, 2, 3, 4, 5, 6, 7));
}
template <int N> __device__ __forceinline__ void wait_vm() { asm volatile("s_waitcnt vmcnt(%0)" ::"n"(N) : "memory"); }
__device__ __forceinline__ void wait_lgkm() { asm volatile("s_waitcnt lgkmcnt(0)" ::: "memory"); }
__device__ __forceinline__ void bar() { asm volatile("s_waitcnt lgkmcnt(0)\n\ts_barrier" ::: "memory"); }
__device__ __forceinline__ u32 lds_peek(lds_t L, int word) { return __builtin_amdgcn_readfirstlane(*(volatile LAS u32*)(L + L_CTL + 4 * word)); }
__device__ __forceinline__ void lds_wait_ge(lds_t L, int word, u32 want) {
#pragma unroll 1
  for (u32 sp = 0; sp < SPIN_L; ++sp) if (lds_peek(L, word) >= want) break;
  asm volatile("" ::: "memory");
}
__device__ __forceinline__ void lds_set(lds_t L, int word) { *(volatile LAS u32*)(L + L_CTL + 4 * word) = 1u; }
__device__ __forceinline__ void lds_add(lds_t L, int word, int lane) {
  if (lane == 0) (void)__hip_atomic_fetch_add((LAS u32*)(L + L_CTL + 4 * word), 1u, __ATOMIC_RELAXED, __HIP_MEMORY_SCOPE_WORKGROUP);
}
__device__ __forceinline__ void flag_sync(lds_t L, int cw, int lane) {
  *(volatile LAS u32*)(L + L_CTL + 4 * (C_FLG + cw)) = 1u;
#pragma unroll 1
  for (u32 sp = 0; sp < SPIN_L; ++sp) {
    const u32 v = *(volatile LAS u32*)(L + L_CTL + 4 * (C_FLG + (lane & 7)));
    if (__builtin_amdgcn_ballot_w64(v == 0u) == 0ull) break;
  }
  asm volatile("" ::: "memory");
}

#define STAMP(i) do {} while (0)

typedef __amdgpu_buffer_rsrc_t rsrc_t;
__device__ __forceinline__ rsrc_t mkrs(const void* p, u32 bytes) { return __builtin_amdgcn_make_buffer_rsrc((void*)p, 0, (int)bytes, RSRC_W3); }
__device__ __forceinline__ v4u ld16(rsrc_t rs, int off) { return __builtin_amdgcn_raw_buffer_load_b128(rs, off, 0, SC1); }
__device__ __forceinline__ v2u ld8(rsrc_t rs, int off) { return __builtin_amdgcn_raw_buffer_load_b64(rs, off, 0, SC1); }
__device__ __forceinline__ void st16(rsrc_t rs, int off, v4u v) { __builtin_amdgcn_raw_buffer_store_b128(v, rs, off, 0, SC1); }
__device__ __forceinline__ void st8(rsrc_t rs, int off, v2u v) { __builtin_amdgcn_raw_buffer_store_b64(v, rs, off, 0, SC1); }
__device__ __forceinline__ u32 mx4(v4u v) { return umax(umax(v.x, v.y), umax(v.z, v.w)); }
__device__ __forceinline__ u32 mx2(v2u v) { return umax(v.x, v.y); }
__device__ __forceinline__ bool wave_full(u32 m) { return __builtin_amdgcn_ballot_w64(m == EMPTY) == 0ull; }
__device__ __forceinline__ v4u f2u(f4 v) { return v4u{fsane(v.x), fsane(v.y), fsane(v.z), fsane(v.w)}; }
__device__ __forceinline__ f4 u2f(v4u v) { return __builtin_bit_cast(f4, v); }
constexpr v4u E4 = {EMPTY, EMPTY, EMPTY, EMPTY};
constexpr v2u E2 = {EMPTY, EMPTY};

#define MK_RSLEEP 4
template <int N> __device__ __forceinline__ void gather16(rsrc_t rs, const int (&off)[N], v4u (&v)[N]) {
#pragma unroll 1
  for (u32 sp = 0; sp < SPIN_G; ++sp) {
#pragma unroll
    for (int i = 0; i < N; ++i) v[i] = ld16(rs, off[i]);
    u32 m = mx4(v[0]);
#pragma unroll
    for (int i = 1; i < N; ++i) m = umax(m, mx4(v[i]));
    if (wave_full(m)) break;
    __builtin_amdgcn_s_sleep(MK_RSLEEP);
  }
}
template <int N> __device__ __forceinline__ void gather8(rsrc_t rs, const int (&off)[N], v2u (&v)[N]) {
#pragma unroll 1
  for (u32 sp = 0; sp < SPIN_G; ++sp) {
#pragma unroll
    for (int i = 0; i < N; ++i) v[i] = ld8(rs, off[i]);
    u32 m = mx2(v[0]);
#pragma unroll
    for (int i = 1; i < N; ++i) m = umax(m, mx2(v[i]));
    if (wave_full(m)) break;
    __builtin_amdgcn_s_sleep(MK_RSLEEP);
  }
}
__device__ __forceinline__ void wait_ready(rsrc_t rs, int lane, u64 ep) {
#pragma unroll 1
  for (u32 sp = 0; sp < SPIN_G; ++sp) {
    bool ok = true;
#pragma unroll
    for (int i = 0; i < 2; ++i) {
      const v4u v = ld16(rs, WS_READY + i * 1024 + lane * 16);
      ok &= (v.x == (u32)ep) & (v.y == (u32)(ep >> 32)) & (v.z == (u32)ep) & (v.w == (u32)(ep >> 32));
    }
    if (__builtin_amdgcn_ballot_w64(!ok) == 0ull) break;
    __builtin_amdgcn_s_sleep(8);
  }
}

__device__ __forceinline__ void dma1(u32 lds, int voff, rsrc_t rs, u32 soff) {
  asm volatile("s_mov_b32 m0, %0\n\ts_nop 0\n\tbuffer_load_dwordx4 %1, %2, %3 offen nt lds" ::"s"(lds), "v"(voff), "s"(rs), "s"(soff) : "memory");
}
struct Loader {
  lds_t L;
  u32 lbase;
  int lane, pend;
  template <int PITCH, bool PACE = true> __device__ __forceinline__ void seg(rsrc_t rs, u32 stride, u32 sbase, u32 ldst, int n, int flag) {
    int vo[4];
    if constexpr (PITCH == 256) {
#pragma unroll
      for (int j = 0; j < 4; ++j) vo[j] = (lane >> 4) * stride + 16 * ((lane & 15) ^ (((lane >> 4) << 2) | j));
    } else {
#pragma unroll
      for (int j = 0; j < 4; ++j) { const int f = ((lane >> 4) & 1) | ((j & 1) << 1); vo[j] = (lane >> 3) * stride + 32 * (((lane & 7) >> 1) ^ f) + 16 * (lane & 1); }
    }
    constexpr u32 RPI = PITCH == 256 ? 4 : 8;
    u32 so = sbase, ld = lbase + ldst;
#pragma unroll 1
    for (int j = 0; j < n; j += 4) {
#pragma unroll
      for (int i = 0; i < 4; ++i) { dma1(ld, vo[i], rs, so); so += RPI * stride; ld += 1024; }
      if constexpr (PACE) {
        wait_vm<NINF>();
        if (pend >= 0 && j + 4 >= NINF) { lds_set(L, C_FULL + pend); pend = -1; }
      }
    }
    if (flag >= 0) pend = flag;
  }
  __device__ __forceinline__ void drain() { wait_vm<0>(); if (pend >= 0) lds_set(L, C_FULL + pend); pend = -1; }
};
enum { F_MAC = 0, F_GA, F_GB, F_SB = 0, F_CB };
enum { G_MAC = 0, G_GA, G_R };

template <int PITCH> __device__ __forceinline__ void tr_base(int lane, int c, int& a0, int& a1) {
  const int g = lane >> 4, q = (lane & 15) >> 2, p = lane & 3;
  if constexpr (PITCH == 256) {
    a0 = 256 * (8 * g + q) + 16 * ((2 * c + (p >> 1)) ^ ((q << 2) | ((2 * g) & 3))) + 8 * (p & 1);
    a1 = 256 * (8 * g + 4 + q) + 16 * ((2 * c + (p >> 1)) ^ ((q << 2) | ((2 * g + 1) & 3))) + 8 * (p & 1);
  } else {
    const int f = (q >> 1) | ((g & 1) << 1);
    a0 = 128 * (8 * g + q) + 32 * (c ^ f) + 8 * p;
    a1 = a0 + 128 * 4;
  }
}
template <int PITCH, int KS> __device__ __forceinline__ void load_w(lds_t a0, lds_t a1, v4u (&w)[KS]) {
#pragma unroll
  for (int s = 0; s < KS; ++s) w[s] = tr2(a0 + s * 32 * PITCH, a1 + s * 32 * PITCH);
}
template <int KS> __device__ __forceinline__ void load_x(lds_t xf, v4u (&x)[KS]) {
#pragma unroll
  for (int s = 0; s < KS; ++s) x[s] = *(LAS v4u*)(xf + s * 64);
}
template <int KS> __device__ __forceinline__ f4 mma(const v4u (&w)[KS], const v4u (&x)[KS]) {
  f4 a = {0.f, 0.f, 0.f, 0.f}, b = {0.f, 0.f, 0.f, 0.f};
#pragma unroll
  for (int s = 0; s < KS; s += 2) { a = mfma(w[s], x[s], a); b = mfma(w[s + 1], x[s + 1], b); }
  return a + b;
}

__device__ __forceinline__ float silu_f(float x) { return x * __builtin_amdgcn_rcpf(1.f + __builtin_amdgcn_exp2f(-LOG2E * x)); }
__device__ __forceinline__ float gelu_f(float x) {
  const float u = (2.f * 0.7978845608028654f * LOG2E) * (x + 0.044715f * x * x * x);
  return x * (1.f - __builtin_amdgcn_rcpf(__builtin_amdgcn_exp2f(u) + 1.f));
}
__device__ __forceinline__ void ln_fin(float mean, float var, float& m, float& rden) {
  m = rbf(mean);
  rden = __builtin_amdgcn_rcpf(rbf(__builtin_amdgcn_sqrtf(rbf(rbf(var) + 1e-5f))));
}

__global__ void __launch_bounds__(NTHR, 3) mega(Params p) {
  extern __shared__ __attribute__((aligned(16))) unsigned char smem[];
  lds_t L = (lds_t)smem;
  const int tid = threadIdx.x, lane = tid & 63, wave = __builtin_amdgcn_readfirstlane(tid >> 6);
  const int bid = blockIdx.x, isY = (bid >> 3) & 1, gi = (bid >> 4) * 8 + (bid & 7);
  const int l15 = lane & 15, g4 = lane >> 4;

  if (wave == 0) {
    const int cw = 0; (void)cw;
    Loader ld{L, (u32)(size_t)smem, lane, -1};
    STAMP(16);
    const rsrc_t rwm = mkrs(p.wm, 1024u * 6144u * 2u);
    if (!isY) {
      const int x = gi;
      const u32 kvo = (u32)((x & 7) * 32768 + 28672 + 256 * (x >> 3)) * 256u;
      const rsrc_t rk = mkrs(p.kc, 8u * 32768u * 256u), rv = mkrs(p.vc, 8u * 32768u * 256u);
      ld.seg<256, false>(rk, 256, kvo, 0, 16, -1);
      bar();
      ld.seg<256>(rk, 256, kvo + 64 * 256, 16384, 48, -1);
      STAMP(17);
      ld.seg<256>(rv, 256, kvo, 65536, 64, -1);
      ld.drain();
      STAMP(18);
      bar();
      bar();
      STAMP(11);
      bar();
      bar();
      STAMP(12);
      STAMP(19);
      const u32 mo = (u32)(128 * (x >> 4)) * 12288u + (u32)(x & 15) * 128u;
      ld.seg<128>(rwm, 12288, mo + 3072 * 2, 0, 16, F_SB);
      ld.seg<128>(rwm, 12288, mo + 4096 * 2, 16384, 16, F_CB);
      if (x < 112) {
        const rsrc_t r1 = mkrs(p.w1, 1024u * 3584u * 2u);
        ld.seg<128>(r1, 7168, (u32)(512 * (x & 1)) * 7168u + (u32)(x >> 1) * 128u, 65536, 64, -1);
        ld.drain();
        STAMP(20);
        bar();
        bar();
        bar();
        bar();
      STAMP(13);
      } else ld.drain();
    } else {
      const int y = gi;
      const u32 mco = (u32)(256 * (y >> 5)) * 12288u + (u32)(y & 31) * 128u;
      ld.seg<128, false>(rwm, 12288, mco, 65536, 16, -1);
      bar();
      ld.seg<128>(rwm, 12288, mco + 128 * 12288, 65536 + 16384, 16, F_MAC);
      const rsrc_t rq = mkrs(p.wq, 1024u * 4096u * 2u);
      ld.seg<256>(rq, 8192, (u32)(256 * ((y & 7) >> 1)) * 8192u + (u32)((y >> 3) * 2 + (y & 1)) * 256u, 0, 64, -1);
      ld.drain();
      STAMP(17);
      bar();
      __builtin_amdgcn_s_sleep(2);
      const u32 mo = (u32)(128 * (y >> 4)) * 12288u + (u32)(y & 15) * 128u;
      ld.seg<128>(rwm, 12288, mo + 2048 * 2, L_GT, 16, F_GA);
      const rsrc_t ro = mkrs(p.wo, 4096u * 1024u * 2u);
      const u32 oo = (u32)(128 * ((y & 7) + 8 * (y >> 5))) * 2048u + (u32)((y >> 3) & 3) * 512u;
      lds_wait_ge(L, C_GATE + G_MAC, 4u);
      ld.seg<256>(ro, 2048, oo, 65536, 32, -1);
      ld.seg<256>(ro, 2048, oo + 256, 65536 + 32768, 32, -1);
      ld.drain();
      STAMP(18);
      bar();
      bar();
      STAMP(14);
      if (y < 64) lds_wait_ge(L, C_GATE + G_R, 2u);
      STAMP(19);
      lds_wait_ge(L, C_GATE + G_GA, 4u);
      ld.seg<128>(rwm, 12288, mo + 5120 * 2, L_GT, 16, F_GB);
      if (y < 112) {
        const rsrc_t r2 = mkrs(p.w2, 3584u * 1024u * 2u);
        const u32 o2 = (u32)(128 * (y >> 2)) * 2048u + (u32)(y & 3) * 512u;
        ld.seg<256>(r2, 2048, o2, 0, 32, -1);
        ld.seg<256>(r2, 2048, o2 + 256, 32768, 32, -1);
        ld.drain();
        STAMP(20);
        bar();
        bar();
      } else ld.drain();
    }
    return;
  }

  const int cw = wave - 1, ct = tid - 64;
  const rsrc_t rs = mkrs(p.ws, WS_END);
  if (ct < 64) *(LAS u32*)(L + L_CTL + 4 * ct) = 0u;
  const u64 epoch = __hip_atomic_load((GAS u64*)(p.ws + WS_EPOCH), __ATOMIC_RELAXED, __HIP_MEMORY_SCOPE_AGENT) + 1ull;
  const v2u cin = *(const GAS v2u*)(p.c + (ct >> 8) * 1024 + (ct & 255) * 4);
  const bool pl = l15 < 2;
  const int pub = l15 * 64 + g4 * 16;
  const int xfl = (l15 & 1) * XF_P + g4 * 16;
  lds_t cf_lane = L + L_CF + (l15 & 1) * CF_P + g4 * 16;
  auto silu_to_lds = [&]() {
    *(LAS v2u*)(L + L_CF + (ct >> 8) * CF_P + (ct & 255) * 8) = v2u{pk2(silu_f(bflo(cin.x)), silu_f(bfhi(cin.x))), pk2(silu_f(bflo(cin.y)), silu_f(bfhi(cin.y)))};
  };
  auto raise_flag = [&]() {
    wait_vm<0>();
    u32 old = 0;
    if (lane == 0) old = __hip_atomic_fetch_add((LAS u32*)(L + L_CTL + 4 * C_CLR), 1u, __ATOMIC_RELAXED, __HIP_MEMORY_SCOPE_WORKGROUP);
    if (__builtin_amdgcn_readfirstlane(old) == 7u && lane == 0) __hip_atomic_store((GAS u64*)(p.ws + WS_READY) + bid, epoch, __ATOMIC_RELAXED, __HIP_MEMORY_SCOPE_AGENT);
  };

  if (!isY) {
    const int x = gi, g = x & 7, rec = g * 16 + (x >> 3);
    int o_o3 = WS_O3 + rec * 2048 + ((cw & 1) * 64 + lane) * 16;
    int o_ml = WS_ML + rec * 128 + (lane & 7) * 8;
    int o_sc = WS_PG + (((1 + (cw >> 2)) * 64 + 4 * (x & 15) + (cw & 3)) * 8 + (x >> 4)) * 128 + pub;
    int o_p6 = WS_P6 + ((x >> 1) * 4 + (x & 1) * 2 + (cw >> 2)) * 512 + l15 * 256 + (cw & 3) * 64 + g4 * 16;
    if (cw < 2) st16(rs, o_o3, E4);
    if (cw == 2 && lane < 8) st8(rs, o_ml, E2);
    if (pl) st16(rs, o_sc, E4);
    if (x < 112 && pl) st16(rs, o_p6, E4);
    silu_to_lds();
    bar();
    raise_flag();
    int qoff[2];
    {
      const int base = WS_P2 + (((g >> 2) * 32 + 8 * (g & 3) + cw) * 4) * 512 + (lane & 31) * 16;
      qoff[0] = base + (lane >> 5) * 512; qoff[1] = base + ((lane >> 5) + 2) * 512;
    }
    lds_t kb = L + 256 * (32 * cw + l15) + 16 * (g4 ^ (l15 >> 2));
    lds_t qw = L + L_QF + cw * QF_P + (lane & 31) * 8 + (lane >> 5) * 4;
    lds_t qb = L + L_QF + (l15 & 7) * QF_P + g4 * 16;
    lds_t vb = L + 65536 + 256 * (4 * g4 + (l15 >> 2)) + 16 * ((2 * cw + ((lane & 3) >> 1)) ^ (((l15 >> 2) << 2) | g4)) + 8 * (lane & 1);
    lds_t pfw = L + L_PF + cw * 1024 + lane * 16, pfr = L + L_PF + lane * 16;
    lds_t mww = L + L_MW + l15 * 32 + cw * 4, mwr = L + L_MW + l15 * 32;
    lds_t otw = L + L_OT + (l15 & 7) * QF_P + (16 * cw + 4 * g4) * 2, otr = L + L_OT + (((cw & 1) * 64 + lane) >> 4) * QF_P + l15 * 16;
    PIN(qoff[0]); PIN(qoff[1]); PIN(kb); PIN(qw); PIN(qb); PIN(vb); PIN(pfw); PIN(pfr); PIN(mww); PIN(mwr); PIN(otw); PIN(otr); PIN(o_o3); PIN(o_ml);
    if (cw == 7) { __builtin_amdgcn_s_sleep(64); wait_ready(rs, lane, epoch); lds_set(L, C_RDY); }
    lds_wait_ge(L, C_RDY, 1u);
    bar();
    STAMP(1);
    {
      v4u kf[8], vf[8];
#pragma unroll
      for (int s = 0; s < 4; ++s) { const int ko = 64 * (s ^ (l15 & 3)); kf[s] = *(LAS v4u*)(kb + ko); kf[4 + s] = *(LAS v4u*)(kb + ko + 4096); }
#pragma unroll
      for (int w = 0; w < 8; ++w) vf[w] = tr2(vb + w * 8192, vb + w * 8192 + 4096);
      v4u v[2];
      gather16<2>(rs, qoff, v);
      STAMP(2);
      {
        const f4 a = u2f(v[0]) + u2f(v[1]);
        *(LAS u32*)qw = pk2(sw32(a.x, a.z), sw32(a.y, a.w));
      }
      bar();
      STAMP(11);
      f4 d0 = {0.f, 0.f, 0.f, 0.f}, d1 = {0.f, 0.f, 0.f, 0.f};
#pragma unroll
      for (int s = 0; s < 4; ++s) {
        const v4u qv = *(LAS v4u*)(qb + s * 64);
        d0 = mfma(kf[s], qv, d0);
        d1 = mfma(kf[4 + s], qv, d1);
      }
      float sc[8];
#pragma unroll
      for (int j = 0; j < 2; ++j) {
        const u32 a = pk2(d0[2 * j], d0[2 * j + 1]), b = pk2(d1[2 * j], d1[2 * j + 1]);
        const u32 c = pk2(bflo(a) * 0.08838834764831845f, bfhi(a) * 0.08838834764831845f), d = pk2(bflo(b) * 0.08838834764831845f, bfhi(b) * 0.08838834764831845f);
        sc[2 * j] = bflo(c); sc[2 * j + 1] = bfhi(c); sc[4 + 2 * j] = bflo(d); sc[5 + 2 * j] = bfhi(d);
      }
      float m = fmaxf(fmaxf(fmaxf(sc[0], sc[1]), fmaxf(sc[2], sc[3])), fmaxf(fmaxf(sc[4], sc[5]), fmaxf(sc[6], sc[7])));
      m = mx32(mx16(m));
#pragma unroll
      for (int j = 0; j < 8; ++j) sc[j] = __builtin_amdgcn_exp2f((sc[j] - m) * LOG2E);
      *(LAS v4u*)pfw = v4u{pk2(sc[0], sc[1]), pk2(sc[2], sc[3]), pk2(sc[4], sc[5]), pk2(sc[6], sc[7])};
      {
        float l = ((sc[0] + sc[1]) + (sc[2] + sc[3])) + ((sc[4] + sc[5]) + (sc[6] + sc[7]));
        l = sw16(l, l); l = sw32(l, l);
        if (g4 == 0) { *(LAS float*)mww = m; *(LAS float*)(mww + 512) = l; }
      }
      bar();
      STAMP(3);
      const f4 m0 = *(LAS f4*)mwr, m1 = *(LAS f4*)(mwr + 16);
      f4 d[8];
#pragma unroll
      for (int w = 0; w < 8; ++w) { const f4 z = {0.f, 0.f, 0.f, 0.f}; d[w] = mfma(vf[w], *(LAS v4u*)(pfr + w * 1024), z); }
      m = fmaxf(fmaxf(fmaxf(m0.x, m0.y), fmaxf(m0.z, m0.w)), fmaxf(fmaxf(m1.x, m1.y), fmaxf(m1.z, m1.w)));
      float f[8];
#pragma unroll
      for (int j = 0; j < 4; ++j) { f[j] = __builtin_amdgcn_exp2f((m0[j] - m) * LOG2E); f[4 + j] = __builtin_amdgcn_exp2f((m1[j] - m) * LOG2E); }
      const f4 acc = (((f[0] * d[0] + f[1] * d[1]) + (f[2] * d[2] + f[3] * d[3])) + ((f[4] * d[4] + f[5] * d[5]) + (f[6] * d[6] + f[7] * d[7])));
      if (l15 < 8) *(LAS v2u*)otw = v2u{sane(pk2(acc.x, acc.y)), sane(pk2(acc.z, acc.w))};
      if (cw == 2) {
        const f4 l0 = *(LAS f4*)(mwr + 512), l1 = *(LAS f4*)(mwr + 528);
        const float l = ((f[0] * l0.x + f[1] * l0.y) + (f[2] * l0.z + f[3] * l0.w)) + ((f[4] * l1.x + f[5] * l1.y) + (f[6] * l1.z + f[7] * l1.w));
        if (lane < 8) st8(rs, o_ml, v2u{fsane(m), fsane(l)});
      }
      bar();
      STAMP(12);
      if (cw < 2) st16(rs, o_o3, *(LAS v4u*)otr);
    }
    STAMP(4);
    {
      int a0, a1;
      tr_base<128>(lane, cw & 3, a0, a1);
      v4u xv[4], w[4];
      load_x<4>(cf_lane + (x >> 4) * 256, xv);
      lds_wait_ge(L, C_FULL + F_SB + (cw >> 2), 1u);
      load_w<128, 4>(L + (cw >> 2) * 16384 + a0, L + (cw >> 2) * 16384 + a1, w);
      const f4 a = mma<4>(w, xv);
      if (pl) st16(rs, o_sc, f2u(a));
    }
    STAMP(5);
    if (x < 112) {
      const int b = cw >> 2, k = 256 * (cw & 3) + 4 * lane;
      const int ro = (16 * (cw & 3) + (lane >> 2)) * 128 + b * 32 + (lane & 3) * 8;
      int offs[2] = {WS_SC + ro, WS_SC + ro + 64}, offx[1] = {WS_X1 + ro};
      int a0, a1;
      tr_base<128>(lane, cw & 3, a0, a1);
      lds_t wa0 = L + 65536 + (cw >> 2) * 32768 + a0, wa1 = L + 65536 + (cw >> 2) * 32768 + a1;
      lds_t xr = L + L_XF + xfl + (cw >> 2) * 512, xw = L + L_XF + b * XF_P + (k & 511) * 2;
      lds_t redw = L + L_RED + cw * 8 + (g4 & 1) * 4, redr = L + L_RED + b * 32, x1w = L + L_X1 + b * 2048 + k * 2;
      PIN(offs[0]); PIN(offs[1]); PIN(offx[0]); PIN(wa0); PIN(wa1); PIN(xr); PIN(xw); PIN(redw); PIN(redr); PIN(x1w); PIN(o_p6);
      bar();
      STAMP(6);
      v4u w[8];
      load_w<128, 8>(wa0, wa1, w);
      v2u vs[2], vx[1];
      gather8<2>(rs, offs, vs);
      const float sb[4] = {bflo(vs[0].x), bfhi(vs[0].x), bflo(vs[0].y), bfhi(vs[0].y)};
      const u32 c01 = pk2(1.f + bflo(vs[1].x), 1.f + bfhi(vs[1].x)), c23 = pk2(1.f + bflo(vs[1].y), 1.f + bfhi(vs[1].y));
      const float cb[4] = {bflo(c01), bfhi(c01), bflo(c23), bfhi(c23)};
      gather8<1>(rs, offx, vx);
      STAMP(7);
      float e[4] = {bflo(vx[0].x), bfhi(vx[0].x), bflo(vx[0].y), bfhi(vx[0].y)};
#pragma unroll
      for (int pass = 0; pass < 2; ++pass) {
        float s1 = row_sum16((e[0] + e[1]) + (e[2] + e[3])), s2 = row_sum16((e[0] * e[0] + e[1] * e[1]) + (e[2] * e[2] + e[3] * e[3]));
        float s = sw16(s1, s2);
        s = sw32(s, s);
        *(LAS float*)(redw + pass * 64) = s;
        if (pass == 0) *(LAS v2u*)x1w = vx[0];
        bar();
        const f4 r0 = *(LAS f4*)(redr + pass * 64), r1 = *(LAS f4*)(redr + pass * 64 + 16);
        const float mean = ((r0.x + r0.z) + (r1.x + r1.z)) * (1.f / 1024.f);
        const float var = ((r0.y + r0.w) + (r1.y + r1.w)) * (1.f / 1024.f) - mean * mean;
        float m, rden;
        ln_fin(mean, fmaxf(var, 0.f), m, rden);
        const u32 d01 = pk2(e[0] - m, e[1] - m), d23 = pk2(e[2] - m, e[3] - m);
        u32 z01 = pk2(bflo(d01) * rden, bfhi(d01) * rden), z23 = pk2(bflo(d23) * rden, bfhi(d23) * rden);
        if (pass == 0) {
          const u32 t01 = pk2(bflo(z01) * cb[0], bfhi(z01) * cb[1]), t23 = pk2(bflo(z23) * cb[2], bfhi(z23) * cb[3]);
          z01 = pk2(bflo(t01) + sb[0], bfhi(t01) + sb[1]); z23 = pk2(bflo(t23) + sb[2], bfhi(t23) + sb[3]);
          e[0] = bflo(z01); e[1] = bfhi(z01); e[2] = bflo(z23); e[3] = bfhi(z23);
        } else if (((cw & 3) >> 1) == (x & 1)) *(LAS v2u*)xw = v2u{z01, z23};
      }
      bar();
      STAMP(13);
      v4u xv[8];
      load_x<8>(xr, xv);
      const f4 a = mma<8>(w, xv);
      if (pl) st16(rs, o_p6, f2u(a));
      STAMP(8);
    }
    if (x < 64 && cw == 0) {
      const int ch = lane & 7, hi = lane >> 3;
      const v2u b2v = *(const GAS v2u*)(p.b2 + 16 * x + 4 * (ch & 3));
      int offg[1] = {WS_PG + ((3 * 64 + x) * 8 + hi) * 128 + ch * 16};
      const int pb = WS_P7 + x * 28 * 128 + ch * 16;
      int off[4] = {pb + hi * 128, pb + (hi + 8) * 128, pb + (hi + 16) * 128, pb + (hi < 4 ? hi + 24 : 0) * 128};
      v4u vg[1];
      gather16<1>(rs, offg, vg);
      float gb = rows4(u2f(vg[0]));
      gb = rbf(gb + dpp<ROR8>(gb));
      const int col = 16 * x + 4 * (ch & 3) + g4;
      const float x1 = bflo(*(LAS u16*)(L + L_X1 + (ch >> 2) * 2048 + col * 2));
      const float b2 = g4 == 0 ? bflo(b2v.x) : g4 == 1 ? bfhi(b2v.x) : g4 == 2 ? bflo(b2v.y) : bfhi(b2v.y);
      GAS u16* op = (GAS u16*)p.out + (ch >> 2) * 1024 + col;
      v4u v[4];
      gather16<4>(rs, off, v);
      STAMP(9);
      f4 a = (u2f(v[0]) + u2f(v[1])) + u2f(v[2]);
      if (hi < 4) a += u2f(v[3]);
      float s = rows4(a);
      s += dpp<ROR8>(s);
      const float o = x1 + rbf(gb * rbf(rbf(s) + b2));
      if ((lane & 8) == 0) *op = (u16)pk2(o, o);
      if (x == 0 && lane == 0) __hip_atomic_store((GAS u64*)(p.ws + WS_EPOCH), epoch, __ATOMIC_RELAXED, __HIP_MEMORY_SCOPE_AGENT);
      STAMP(10);
    }
  } else {
    const int y = gi;
    const int qks = (y & 7) >> 1, qh = (y >> 3) * 2 + (y & 1);
    const int oh = (y & 7) + 8 * (y >> 5), ons = (y >> 3) & 3;
    int o_mac = WS_P1 + ((y & 31) * 4 + (y >> 5)) * 512 + l15 * 256 + (cw & 3) * 64 + g4 * 16;
    int o_p2 = WS_P2 + ((l15 * 32 + qh) * 4 + qks) * 512 + cw * 64 + g4 * 16;
    int o_g = WS_PG + ((4 * (y & 15) + (cw & 3)) * 8 + (y >> 4)) * 128 + pub;
    const int rblk = (cw >> 2) * 8 + 2 * (cw & 3);
    int o_p4 = WS_P4 + ((ons * 16 + rblk) * 32 + oh) * 128 + pub;
    int o_p7 = WS_P7 + (((y & 3) * 16 + rblk) * 28 + (y >> 2)) * 128 + pub;
    int o_r = (cw == 0 ? WS_X1 : WS_SC) + y * 128 + lane * 8;
    if (pl) {
      if (cw < 4) st16(rs, o_mac, E4);
      else { st16(rs, o_g, E4); st16(rs, o_g + 3 * 64 * 8 * 128, E4); }
      st16(rs, o_p2, E4);
      st16(rs, o_p4, E4); st16(rs, o_p4 + 32 * 128, E4);
      if (y < 112) { st16(rs, o_p7, E4); st16(rs, o_p7 + 28 * 128, E4); }
    }
    if (y < 64 && ((cw == 0 && lane < 8) || (cw == 1 && lane < 16))) st8(rs, o_r, E2);
    const GAS v4u* xg = (const GAS v4u*)p.x;
    const v4u xa0 = xg[lane * 2], xa1 = xg[lane * 2 + 1], xb0 = xg[128 + lane * 2], xb1 = xg[128 + lane * 2 + 1];
    const int hb = (lane >> 3) & 1, hk = 32 * cw + 4 * (lane & 7) + g4;
    const u32 xq = ((const GAS u16*)p.x)[hb * 1024 + 256 * qks + hk];
    silu_to_lds();
    bar();
    raise_flag();
    if (cw < 4) {
      int a0, a1;
      tr_base<128>(lane, cw, a0, a1);
      v4u xv[8], w[8];
      load_x<8>(cf_lane + (y >> 5) * 512, xv);
      lds_t wa0 = L + 65536 + a0, wa1 = L + 65536 + a1;
      PIN(wa0); PIN(wa1); PIN(o_mac);
      lds_wait_ge(L, C_FULL + F_MAC, 1u);
      STAMP(1);
      load_w<128, 8>(wa0, wa1, w);
      const f4 a = mma<8>(w, xv);
      if (pl) st16(rs, o_mac, f2u(a));
      lds_add(L, C_GATE + G_MAC, lane);
      STAMP(2);
    }
    float ln;
    {
      float m[2], rd[2];
#pragma unroll
      for (int b = 0; b < 2; ++b) {
        const v4u u0 = b ? xb0 : xa0, u1 = b ? xb1 : xa1;
        float e[16];
#pragma unroll
        for (int j = 0; j < 4; ++j) { e[2 * j] = bflo(u0[j]); e[2 * j + 1] = bfhi(u0[j]); e[8 + 2 * j] = bflo(u1[j]); e[9 + 2 * j] = bfhi(u1[j]); }
        float s = 0.f;
#pragma unroll
        for (int j = 0; j < 16; ++j) s += e[j];
        s = row_sum16(s); s = sw16(s, s); s = sw32(s, s);
        const float mean = s * (1.f / 1024.f);
        float s2 = 0.f;
#pragma unroll
        for (int j = 0; j < 16; ++j) { const float d = e[j] - mean; s2 += d * d; }
        s2 = row_sum16(s2); s2 = sw16(s2, s2); s2 = sw32(s2, s2);
        ln_fin(mean, s2 * (1.f / 1024.f), m[b], rd[b]);
      }
      ln = rbf(rbf(bflo(xq) - (hb ? m[1] : m[0])) * (hb ? rd[1] : rd[0]));
    }
    {
      int off[2];
      off[0] = WS_P1 + ((4 * qks + (cw >> 1)) * 4 + g4) * 512 + hb * 256 + (32 * (cw & 1) + 4 * (lane & 7)) * 4;
      off[1] = off[0] + 16 * 4 * 512;
      int a0, a1;
      tr_base<256>(lane, cw, a0, a1);
      lds_t wa0 = L + a0, wa1 = L + a1, xw = L + L_XF + hb * XF_P + hk * 2, xr = L + L_XF + xfl;
      PIN(ln); PIN(off[0]); PIN(off[1]); PIN(wa0); PIN(wa1); PIN(xw); PIN(xr); PIN(o_p2);
      if (cw == 7) { wait_ready(rs, lane, epoch); lds_set(L, C_RDY); }
      lds_wait_ge(L, C_RDY, 1u);
      bar();
      STAMP(3);
      v4u v[2], w[8];
      v[0] = ld16(rs, off[0]); v[1] = ld16(rs, off[1]);
      SCHED();
      load_w<256, 8>(wa0, wa1, w);
      if (!wave_full(umax(mx4(v[0]), mx4(v[1])))) gather16<2>(rs, off, v);
      STAMP(4);
      const u32 sc = pk2(rows4(u2f(v[0])), rows4(u2f(v[1])));
      const float h = rbf(ln * rbf(1.f + bfhi(sc))) + bflo(sc);
      *(LAS u16*)xw = (u16)pk2(h, h);
      flag_sync(L, cw, lane);
      STAMP(15);
      v4u xv[8];
      load_x<8>(xr, xv);
      const f4 a = mma<8>(w, xv);
      if (pl) st16(rs, o_p2, f2u(a));
      STAMP(5);
    }
    int ga0, ga1;
    tr_base<128>(lane, cw & 3, ga0, ga1);
    if (cw >= 4) {
      v4u xv[4], w[4];
      load_x<4>(cf_lane + (y >> 4) * 256, xv);
      lds_wait_ge(L, C_FULL + F_GA, 1u);
      load_w<128, 4>(L + L_GT + ga0, L + L_GT + ga1, w);
      const f4 a = mma<4>(w, xv);
      if (pl) st16(rs, o_g, f2u(a));
      lds_add(L, C_GATE + G_GA, lane);
    }
    int ta0, ta1, tb0, tb1;
    tr_base<256>(lane, 2 * (cw & 3), ta0, ta1);
    tr_base<256>(lane, 2 * (cw & 3) + 1, tb0, tb1);
    {
      const int b = (cw >> 1) & 1, dh = cw & 1, g = b * 4 + (oh >> 3), hh = oh & 7, s0 = lane >> 3;
      int ob = WS_O3 + ((g * 16 + s0) * 8 + hh) * 256 + dh * 128 + (lane & 7) * 16;
      int mb = WS_ML + (g * 16 + s0) * 128 + hh * 8;
      lds_t tile = L + 65536 + (cw >> 2) * 32768;
      lds_t pa0 = tile + ta0, pa1 = tile + ta1, pb0 = tile + tb0, pb1 = tile + tb1;
      lds_t xw = L + L_XF + 2 * XF_P + b * XF_P + (64 * dh + 8 * (lane & 7) + 2 * g4) * 2, xr = L + L_XF + 2 * XF_P + xfl;
      PIN(ob); PIN(mb); PIN(pa0); PIN(pa1); PIN(pb0); PIN(pb1); PIN(xw); PIN(xr); PIN(o_p4);
      bar();
      STAMP(6);
      v4u wa[4], wb[4];
      load_w<256, 4>(pa0, pa1, wa);
      load_w<256, 4>(pb0, pb1, wb);
      if (cw < 4) {
        v4u v[2]; v2u ml[2];
#pragma unroll 1
        for (u32 sp = 0; sp < SPIN_G; ++sp) {
          ml[0] = ld8(rs, mb); ml[1] = ld8(rs, mb + 8 * 128);
          v[0] = ld16(rs, ob); v[1] = ld16(rs, ob + 8 * 2048);
          if (wave_full(umax(umax(mx2(ml[0]), mx2(ml[1])), umax(mx4(v[0]), mx4(v[1]))))) break;
          __builtin_amdgcn_s_sleep(MK_RSLEEP);
        }
        STAMP(7);
        const float m0 = asf(ml[0].x), m1 = asf(ml[1].x);
        float M = fmaxf(m0, m1);
        M = fmaxf(M, dpp<ROR8>(M));
        M = mx32(mx16(M));
        const float e0 = __builtin_amdgcn_exp2f((m0 - M) * LOG2E), e1 = __builtin_amdgcn_exp2f((m1 - M) * LOG2E);
        float Ls = e0 * asf(ml[0].y) + e1 * asf(ml[1].y);
        Ls += dpp<ROR8>(Ls);
        Ls = sw16(Ls, Ls); Ls = sw32(Ls, Ls);
        float o[8];
#pragma unroll
        for (int j = 0; j < 4; ++j) {
          o[2 * j] = e0 * bflo(v[0][j]) + e1 * bflo(v[1][j]);
          o[2 * j + 1] = e0 * bfhi(v[0][j]) + e1 * bfhi(v[1][j]);
        }
        float t0 = sw16(sw32(o[0], o[4]), sw32(o[2], o[6])), t1 = sw16(sw32(o[1], o[5]), sw32(o[3], o[7]));
        t0 += dpp<ROR8>(t0); t1 += dpp<ROR8>(t1);
        const float inv = __builtin_amdgcn_rcpf(Ls);
        if ((lane & 8) == 0) *(LAS u32*)xw = pk2(t0 * inv, t1 * inv);
      }
      bar();
      STAMP(14);
      v4u xv[4];
      load_x<4>(xr, xv);
      const f4 a0 = mma<4>(wa, xv), a1 = mma<4>(wb, xv);
      if (pl) { st16(rs, o_p4, f2u(a0)); st16(rs, o_p4 + 32 * 128, f2u(a1)); }
      STAMP(8);
    }
    if (y < 64 && cw < 2) {
      const int ch = lane & 7, hi = lane >> 3;
      if (cw == 0) {
        const u32 xv = *(const GAS u32*)(p.x + (ch >> 2) * 1024 + 16 * y + 4 * (ch & 3) + 2 * (lane >> 5));
        int offg[1] = {WS_PG + ((0 * 64 + y) * 8 + hi) * 128 + ch * 16};
        const int pb = WS_P4 + (y * 32 + hi) * 128 + ch * 16;
        int off[4] = {pb, pb + 8 * 128, pb + 16 * 128, pb + 24 * 128};
        int ost = WS_X1 + y * 128 + ch * 8 + (lane >> 5) * 4;
        v4u vg[1];
        gather16<1>(rs, offg, vg);
        const f4 gg = u2f(vg[0]);
        float g0 = sw32(gg.x, gg.z), g1 = sw32(gg.y, gg.w);
        g0 = sw16(g0, g0); g1 = sw16(g1, g1);
        g0 = rbf(g0 + dpp<ROR8>(g0)); g1 = rbf(g1 + dpp<ROR8>(g1));
        PIN(g0); PIN(g1); PIN(ost);
        v4u v[4];
        gather16<4>(rs, off, v);
        STAMP(9);
        const f4 a = (u2f(v[0]) + u2f(v[1])) + (u2f(v[2]) + u2f(v[3]));
        float s0 = sw32(a.x, a.z), s1 = sw32(a.y, a.w);
        s0 = sw16(s0, s0); s1 = sw16(s1, s1);
        const u32 at = pk2(s0 + dpp<ROR8>(s0), s1 + dpp<ROR8>(s1));
        const u32 t = pk2(g0 * bflo(at), g1 * bfhi(at));
        const u32 o = sane(pk2(bflo(xv) + bflo(t), bfhi(xv) + bfhi(t)));
        if ((lane & 24) == 0) __builtin_amdgcn_raw_buffer_store_b32(o, rs, ost, 0, SC1);
        STAMP(10);
      } else {
        int off[2] = {WS_PG + ((1 * 64 + y) * 8 + hi) * 128 + ch * 16, WS_PG + ((2 * 64 + y) * 8 + hi) * 128 + ch * 16};
        v4u v[2];
        gather16<2>(rs, off, v);
        const f4 a = u2f(v[0]), c = u2f(v[1]);
        float s0 = sw16(sw32(a.x, a.z), sw32(c.x, c.z)), s1 = sw16(sw32(a.y, a.w), sw32(c.y, c.w));
        s0 += dpp<ROR8>(s0); s1 += dpp<ROR8>(s1);
        const int ost = WS_SC + y * 128 + (g4 & 1) * 64 + ch * 8 + (lane >> 5) * 4;
        if ((lane & 8) == 0) __builtin_amdgcn_raw_buffer_store_b32(sane(pk2(s0, s1)), rs, ost, 0, SC1);
      }
      lds_add(L, C_GATE + G_R, lane);
    }
    if (cw >= 4) {
      v4u xv[4], w[4];
      load_x<4>(cf_lane + (y >> 4) * 256, xv);
      lds_wait_ge(L, C_FULL + F_GB, 1u);
      load_w<128, 4>(L + L_GT + ga0, L + L_GT + ga1, w);
      const f4 a = mma<4>(w, xv);
      if (pl) st16(rs, o_g + 3 * 64 * 8 * 128, f2u(a));
    }
    if (y < 112) {
      const int fs = y >> 2;
      const int id = 16 * (cw & 3) + l15, nsl = id >> 5, b = (id >> 4) & 1, quad = id & 15;
      float b1v = bflo(((const GAS u16*)p.b1)[128 * fs + 64 * nsl + 4 * quad + g4]);
      int off[1] = {WS_P6 + ((2 * fs + nsl) * 4 + g4) * 512 + b * 256 + quad * 16};
      lds_t tile = L + (cw >> 2) * 32768;
      lds_t pa0 = tile + ta0, pa1 = tile + ta1, pb0 = tile + tb0, pb1 = tile + tb1;
      lds_t xw = L + L_XF + 4 * XF_P + b * XF_P + (64 * nsl + 4 * quad + g4) * 2, xr = L + L_XF + 4 * XF_P + xfl;
      PIN(b1v); PIN(off[0]); PIN(pa0); PIN(pa1); PIN(pb0); PIN(pb1); PIN(xw); PIN(xr); PIN(o_p7);
      bar();
      STAMP(11);
      v4u wa[4], wb[4];
      load_w<256, 4>(pa0, pa1, wa);
      load_w<256, 4>(pb0, pb1, wb);
      if (cw < 4) {
        v4u v[1];
        gather16<1>(rs, off, v);
        STAMP(12);
        const float hv = gelu_f(rbf(rbf(rows4(u2f(v[0]))) + b1v));
        *(LAS u16*)xw = (u16)pk2(hv, hv);
      }
      bar();
      v4u xv[4];
      load_x<4>(xr, xv);
      const f4 a0 = mma<4>(wa, xv), a1 = mma<4>(wb, xv);
      if (pl) { st16(rs, o_p7, f2u(a0)); st16(rs, o_p7 + 28 * 128, f2u(a1)); }
      STAMP(13);
    }
  }
}
}

extern "C" void kernel_launch(void* const* d_in, const int* in_sizes, int n_in, void* d_out, int out_size, void* d_ws, size_t ws_size, hipStream_t stream) {
  using namespace mk;
  Params p;
  p.x = (const u16*)d_in[0]; p.kc = (const u16*)d_in[1]; p.vc = (const u16*)d_in[2]; p.wq = (const u16*)d_in[3]; p.wo = (const u16*)d_in[4];
  p.w1 = (const u16*)d_in[6]; p.b1 = (const u16*)d_in[7]; p.w2 = (const u16*)d_in[8]; p.b2 = (const u16*)d_in[9];
  p.c = (const u16*)d_in[10]; p.wm = (const u16*)d_in[11];
  p.out = (u16*)d_out; p.ws = (unsigned char*)d_ws;
  (void)hipFuncSetAttribute((const void*)mega, hipFuncAttributeMaxDynamicSharedMemorySize, LDS_BYTES);
  hipLaunchKernelGGL(mega, dim3(NWG), dim3(NTHR), LDS_BYTES, stream, p);
}
```

```cpp
#define PREK 0
#define POSTK 1
#include <hip/hip_runtime.h>
#include <stdint.h>
#pragma clang fp contract(off)

namespace mk {
#define GAS __attribute__((address_space(1)))
#define LAS __attribute__((address_space(3)))
typedef unsigned short u16;
typedef unsigned u32;
typedef unsigned long long u64;
typedef u32 v4u __attribute__((ext_vector_type(4)));
typedef u32 v2u __attribute__((ext_vector_type(2)));
typedef float f4 __attribute__((ext_vector_type(4)));
typedef float f2 __attribute__((ext_vector_type(2)));
typedef short s4 __attribute__((ext_vector_type(4)));
typedef __bf16 bf8 __attribute__((ext_vector_type(8)));
typedef __bf16 bf2 __attribute__((ext_vector_type(2)));
typedef LAS unsigned char* lds_t;

constexpr int NWG = 256, NTHR = 576;
constexpr u32 EMPTY = 0xFFFFFFFFu;
constexpr int RSRC_W3 = 0x00020000;
constexpr int SC1 = 16;
constexpr u32 SPIN_G = 1u << 17;
constexpr u32 SPIN_L = 1u << 22;
constexpr float LOG2E = 1.4426950408889634f;

constexpr int WS_EPOCH = 0, WS_READY = 256;
constexpr int WS_P1 = 4096;
constexpr int WS_P2 = WS_P1 + 128 * 512;
constexpr int WS_O3 = WS_P2 + 256 * 512;
constexpr int WS_ML = WS_O3 + 128 * 2048;
constexpr int WS_P4 = WS_ML + 128 * 128;
constexpr int WS_PG = WS_P4 + 64 * 32 * 128;
constexpr int WS_X1 = WS_PG + 4 * 64 * 8 * 128;
constexpr int WS_SC = WS_X1 + 64 * 128;
constexpr int WS_P6 = WS_SC + 64 * 128;
constexpr int WS_P7 = WS_P6 + 56 * 2048;
constexpr int WS_END = WS_P7 + 64 * 28 * 128;
constexpr int WS_STAMP = 4 << 20;

constexpr int L_CTL = 131072;
constexpr int L_STAMP = L_CTL + 256;
constexpr int L_CF = L_STAMP + 1280;
constexpr int CF_P = 2048 + 16;
constexpr int L_XF = L_CF + 2 * CF_P;
constexpr int XF_P = 1024 + 16;
constexpr int L_G = L_XF + 6 * XF_P;
constexpr int QF_P = 256 + 16;
constexpr int L_QF = L_G;
constexpr int L_PF = L_QF + 8 * QF_P;
constexpr int L_MW = L_PF + 8192;
constexpr int L_OT = L_MW + 1024;
constexpr int L_RED = L_OT + 8 * QF_P;
constexpr int L_X1 = L_RED + 256;
constexpr int L_XEND = L_X1 + 4096;
constexpr int L_GT = L_G;
constexpr int L_YEND = L_GT + 16384;
constexpr int LDS_BYTES = 160 * 1024;
static_assert(L_XEND <= LDS_BYTES && L_YEND <= LDS_BYTES, "lds");
static_assert((L_CF % 16) == 0 && (L_XF % 16) == 0 && (L_G % 16) == 0 && (L_PF % 16) == 0 && (L_MW % 16) == 0 && (L_OT % 16) == 0 && (L_RED % 16) == 0 && (L_X1 % 16) == 0, "align");
enum { C_FULL = 0  , C_FLG = 8  , C_GATE = 16  , C_CLR = 24, C_RDY = 25, C_DONE = 26 };
#define MK_NINF 16
constexpr int NINF = MK_NINF;

struct Params {
  const u16 *x, *kc, *vc, *wq, *wo, *w1, *b1, *w2, *b2, *c, *wm;
  u16* out;
  unsigned char* ws;
};

#define PIN(x) asm volatile("" : "+v"(x))
#define SCHED() __builtin_amdgcn_sched_barrier(0)
__device__ __forceinline__ float asf(u32 b) { return __builtin_bit_cast(float, b); }
__device__ __forceinline__ u32 asu(float f) { return __builtin_bit_cast(u32, f); }
__device__ __forceinline__ float bflo(u32 w) { return asf(w << 16); }
__device__ __forceinline__ float bfhi(u32 w) { return asf(w & 0xffff0000u); }
__device__ __forceinline__ u32 pk2(float lo, float hi) { bf2 v = {(__bf16)lo, (__bf16)hi}; return __builtin_bit_cast(u32, v); }
__device__ __forceinline__ float rbf(float f) { return bflo(pk2(f, f)); }
__device__ __forceinline__ u32 umin(u32 a, u32 b) { return a < b ? a : b; }
__device__ __forceinline__ u32 umax(u32 a, u32 b) { return a > b ? a : b; }
__device__ __forceinline__ u32 sane(u32 w) { return umin(w, 0xFFFEFFFFu); }
__device__ __forceinline__ u32 fsane(float f) { return umin(asu(f), 0xFFFFFFFEu); }
template <int CTRL> __device__ __forceinline__ float dpp(float x) { return __builtin_bit_cast(float, __builtin_amdgcn_mov_dpp(__builtin_bit_cast(int, x), CTRL, 0xf, 0xf, true)); }
constexpr int XOR1 = 0xB1, XOR2 = 0x4E, XOR7 = 0x141, ROR8 = 0x128;
__device__ __forceinline__ float sw32(float a, float b) { auto s = __builtin_amdgcn_permlane32_swap(asu(a), asu(b), false, false); return asf((u32)s[0]) + asf((u32)s[1]); }
__device__ __forceinline__ float sw16(float a, float b) { auto s = __builtin_amdgcn_permlane16_swap(asu(a), asu(b), false, false); return asf((u32)s[0]) + asf((u32)s[1]); }
__device__ __forceinline__ float mx32(float a) { auto s = __builtin_amdgcn_permlane32_swap(asu(a), asu(a), false, false); return fmaxf(asf((u32)s[0]), asf((u32)s[1])); }
__device__ __forceinline__ float mx16(float a) { auto s = __builtin_amdgcn_permlane16_swap(asu(a), asu(a), false, false); return fmaxf(asf((u32)s[0]), asf((u32)s[1])); }
__device__ __forceinline__ float rows4(f4 v) { return sw16(sw32(v.x, v.z), sw32(v.y, v.w)); }
__device__ __forceinline__ float row_sum16(float x) { x += dpp<XOR1>(x); x += dpp<XOR2>(x); x += dpp<XOR7>(x); x += dpp<ROR8>(x); return x; }
__device__ __forceinline__ f4 mfma(v4u a, v4u b, f4 c) { return __builtin_amdgcn_mfma_f32_16x16x32_bf16(__builtin_bit_cast(bf8, a), __builtin_bit_cast(bf8, b), c, 0, 0, 0); }
__device__ __forceinline__ v4u tr2(lds_t a0, lds_t a1) {
  s4 r0 = __builtin_amdgcn_ds_read_tr16_b64_v4i16((LAS s4*)a0), r1 = __builtin_amdgcn_ds_read_tr16_b64_v4i16((LAS s4*)a1);
  return __builtin_bit_cast(v4u, __builtin_shufflevector(r0, r1, 0, 1, 2, 3, 4, 5, 6, 7));
}
template <int N> __device__ __forceinline__ void wait_vm() { asm volatile("s_waitcnt vmcnt(%0)" ::"n"(N) : "memory"); }
__device__ __forceinline__ void wait_lgkm() { asm volatile("s_waitcnt lgkmcnt(0)" ::: "memory"); }
__device__ __forceinline__ void bar() { asm volatile("s_waitcnt lgkmcnt(0)\n\ts_barrier" ::: "memory"); }
__device__ __forceinline__ u32 lds_peek(lds_t L, int word) { return __builtin_amdgcn_readfirstlane(*(volatile LAS u32*)(L + L_CTL + 4 * word)); }
__device__ __forceinline__ void lds_wait_ge(lds_t L, int word, u32 want) {
#pragma unroll 1
  for (u32 sp = 0; sp < SPIN_L; ++sp) if (lds_peek(L, word) >= want) break;
  asm volatile("" ::: "memory");
}
__device__ __forceinline__ void lds_set(lds_t L, int word) { *(volatile LAS u32*)(L + L_CTL + 4 * word) = 1u; }
__device__ __forceinline__ void lds_add(lds_t L, int word, int lane) {
  if (lane == 0) (void)__hip_atomic_fetch_add((LAS u32*)(L + L_CTL + 4 * word), 1u, __ATOMIC_RELAXED, __HIP_MEMORY_SCOPE_WORKGROUP);
}
__device__ __forceinline__ void flag_sync(lds_t L, int cw, int lane) {
  *(volatile LAS u32*)(L + L_CTL + 4 * (C_FLG + cw)) = 1u;
#pragma unroll 1
  for (u32 sp = 0; sp < SPIN_L; ++sp) {
    const u32 v = *(volatile LAS u32*)(L + L_CTL + 4 * (C_FLG + (lane & 7)));
    if (__builtin_amdgcn_ballot_w64(v == 0u) == 0ull) break;
  }
  asm volatile("" ::: "memory");
}

#define STAMP(i) do {} while (0)

typedef __amdgpu_buffer_rsrc_t rsrc_t;
__device__ __forceinline__ rsrc_t mkrs(const void* p, u32 bytes) { return __builtin_amdgcn_make_buffer_rsrc((void*)p, 0, (int)bytes, RSRC_W3); }
__device__ __forceinline__ v4u ld16(rsrc_t rs, int off) { return __builtin_amdgcn_raw_buffer_load_b128(rs, off, 0, SC1); }
__device__ __forceinline__ v2u ld8(rsrc_t rs, int off) { return __builtin_amdgcn_raw_buffer_load_b64(rs, off, 0, SC1); }
__device__ __forceinline__ void st16(rsrc_t rs, int off, v4u v) { __builtin_amdgcn_raw_buffer_store_b128(v, rs, off, 0, SC1); }
__device__ __forceinline__ void st8(rsrc_t rs, int off, v2u v) { __builtin_amdgcn_raw_buffer_store_b64(v, rs, off, 0, SC1); }
__device__ __forceinline__ u32 mx4(v4u v) { return umax(umax(v.x, v.y), umax(v.z, v.w)); }
__device__ __forceinline__ u32 mx2(v2u v) { return umax(v.x, v.y); }
__device__ __forceinline__ bool wave_full(u32 m) { return __builtin_amdgcn_ballot_w64(m == EMPTY) == 0ull; }
__device__ __forceinline__ v4u f2u(f4 v) { return v4u{fsane(v.x), fsane(v.y), fsane(v.z), fsane(v.w)}; }
__device__ __forceinline__ f4 u2f(v4u v) { return __builtin_bit_cast(f4, v); }
constexpr v4u E4 = {EMPTY, EMPTY, EMPTY, EMPTY};
constexpr v2u E2 = {EMPTY, EMPTY};

#define MK_RSLEEP 4
template <int N> __device__ __forceinline__ void gather16(rsrc_t rs, const int (&off)[N], v4u (&v)[N]) {
#pragma unroll 1
  for (u32 sp = 0; sp < SPIN_G; ++sp) {
#pragma unroll
    for (int i = 0; i < N; ++i) v[i] = ld16(rs, off[i]);
    u32 m = mx4(v[0]);
#pragma unroll
    for (int i = 1; i < N; ++i) m = umax(m, mx4(v[i]));
    if (wave_full(m)) break;
    __builtin_amdgcn_s_sleep(MK_RSLEEP);
  }
}
template <int N> __device__ __forceinline__ void gather8(rsrc_t rs, const int (&off)[N], v2u (&v)[N]) {
#pragma unroll 1
  for (u32 sp = 0; sp < SPIN_G; ++sp) {
#pragma unroll
    for (int i = 0; i < N; ++i) v[i] = ld8(rs, off[i]);
    u32 m = mx2(v[0]);
#pragma unroll
    for (int i = 1; i < N; ++i) m = umax(m, mx2(v[i]));
    if (wave_full(m)) break;
    __builtin_amdgcn_s_sleep(MK_RSLEEP);
  }
}
__device__ __forceinline__ void wait_ready(rsrc_t rs, int lane, u64 ep) {
#pragma unroll 1
  for (u32 sp = 0; sp < SPIN_G; ++sp) {
    bool ok = true;
#pragma unroll
    for (int i = 0; i < 2; ++i) {
      const v4u v = ld16(rs, WS_READY + i * 1024 + lane * 16);
      ok &= (v.x == (u32)ep) & (v.y == (u32)(ep >> 32)) & (v.z == (u32)ep) & (v.w == (u32)(ep >> 32));
    }
    if (__builtin_amdgcn_ballot_w64(!ok) == 0ull) break;
    __builtin_amdgcn_s_sleep(8);
  }
}

__device__ __forceinline__ void dma1(u32 lds, int voff, rsrc_t rs, u32 soff) {
  asm volatile("s_mov_b32 m0, %0\n\ts_nop 0\n\tbuffer_load_dwordx4 %1, %2, %3 offen nt lds" ::"s"(lds), "v"(voff), "s"(rs), "s"(soff) : "memory");
}
struct Loader {
  lds_t L;
  u32 lbase;
  int lane, pend;
  template <int PITCH, bool PACE = true> __device__ __forceinline__ void seg(rsrc_t rs, u32 stride, u32 sbase, u32 ldst, int n, int flag) {
    int vo[4];
    if constexpr (PITCH == 256) {
#pragma unroll
      for (int j = 0; j < 4; ++j) vo[j] = (lane >> 4) * stride + 16 * ((lane & 15) ^ (((lane >> 4) << 2) | j));
    } else {
#pragma unroll
      for (int j = 0; j < 4; ++j) { const int f = ((lane >> 4) & 1) | ((j & 1) << 1); vo[j] = (lane >> 3) * stride + 32 * (((lane & 7) >> 1) ^ f) + 16 * (lane & 1); }
    }
    constexpr u32 RPI = PITCH == 256 ? 4 : 8;
    u32 so = sbase, ld = lbase + ldst;
#pragma unroll 1
    for (int j = 0; j < n; j += 4) {
#pragma unroll
      for (int i = 0; i < 4; ++i) { dma1(ld, vo[i], rs, so); so += RPI * stride; ld += 1024; }
      if constexpr (PACE) {
        wait_vm<NINF>();
        if (pend >= 0 && j + 4 >= NINF) { lds_set(L, C_FULL + pend); pend = -1; }
      }
    }
    if (flag >= 0) pend = flag;
  }
  __device__ __forceinline__ void drain() { wait_vm<0>(); if (pend >= 0) lds_set(L, C_FULL + pend); pend = -1; }
};
enum { F_MAC = 0, F_GA, F_GB, F_SB = 0, F_CB };
enum { G_MAC = 0, G_GA, G_R };

template <int PITCH> __device__ __forceinline__ void tr_base(int lane, int c, int& a0, int& a1) {
  const int g = lane >> 4, q = (lane & 15) >> 2, p = lane & 3;
  if constexpr (PITCH == 256) {
    a0 = 256 * (8 * g + q) + 16 * ((2 * c + (p >> 1)) ^ ((q << 2) | ((2 * g) & 3))) + 8 * (p & 1);
    a1 = 256 * (8 * g + 4 + q) + 16 * ((2 * c + (p >> 1)) ^ ((q << 2) | ((2 * g + 1) & 3))) + 8 * (p & 1);
  } else {
    const int f = (q >> 1) | ((g & 1) << 1);
    a0 = 128 * (8 * g + q) + 32 * (c ^ f) + 8 * p;
    a1 = a0 + 128 * 4;
  }
}
template <int PITCH, int KS> __device__ __forceinline__ void load_w(lds_t a0, lds_t a1, v4u (&w)[KS]) {
#pragma unroll
  for (int s = 0; s < KS; ++s) w[s] = tr2(a0 + s * 32 * PITCH, a1 + s * 32 * PITCH);
}
template <int KS> __device__ __forceinline__ void load_x(lds_t xf, v4u (&x)[KS]) {
#pragma unroll
  for (int s = 0; s < KS; ++s) x[s] = *(LAS v4u*)(xf + s * 64);
}
template <int KS> __device__ __forceinline__ f4 mma(const v4u (&w)[KS], const v4u (&x)[KS]) {
  f4 a = {0.f, 0.f, 0.f, 0.f}, b = {0.f, 0.f, 0.f, 0.f};
#pragma unroll
  for (int s = 0; s < KS; s += 2) { a = mfma(w[s], x[s], a); b = mfma(w[s + 1], x[s + 1], b); }
  return a + b;
}

__device__ __forceinline__ float silu_f(float x) { return x * __builtin_amdgcn_rcpf(1.f + __builtin_amdgcn_exp2f(-LOG2E * x)); }
__device__ __forceinline__ float gelu_f(float x) {
  const float u = (2.f * 0.7978845608028654f * LOG2E) * (x + 0.044715f * x * x * x);
  return x * (1.f - __builtin_amdgcn_rcpf(__builtin_amdgcn_exp2f(u) + 1.f));
}
__device__ __forceinline__ void ln_fin(float mean, float var, float& m, float& rden) {
  m = rbf(mean);
  rden = __builtin_amdgcn_rcpf(rbf(__builtin_amdgcn_sqrtf(rbf(rbf(var) + 1e-5f))));
}

__global__ void pre_k(unsigned char* ws) { if (threadIdx.x == 0) __hip_atomic_store((GAS u64*)(ws + (5 << 20)), (u64)__builtin_amdgcn_s_memrealtime(), __ATOMIC_RELAXED, __HIP_MEMORY_SCOPE_AGENT); }
__global__ void post_k(unsigned char* ws, int k) {
  const u64 t0 = __builtin_amdgcn_s_memrealtime();
  const u64 te = __hip_atomic_load((GAS u64*)(ws + (6 << 20)), __ATOMIC_RELAXED, __HIP_MEMORY_SCOPE_AGENT);
  u64 d = t0 - te; if (d > 5000) d = 5000;
  const u64 until = __builtin_amdgcn_s_memrealtime() + (u64)k * d;
  for (unsigned sp = 0; sp < (1u << 22); ++sp) if (__builtin_amdgcn_s_memrealtime() >= until) break;
}
__global__ void __launch_bounds__(NTHR, 3) mega(Params p) {
  const u64 t_start0 = __builtin_amdgcn_s_memrealtime();
  extern __shared__ __attribute__((aligned(16))) unsigned char smem[];
  lds_t L = (lds_t)smem;
  const int tid = threadIdx.x, lane = tid & 63, wave = __builtin_amdgcn_readfirstlane(tid >> 6);
  const int bid = blockIdx.x, isY = (bid >> 3) & 1, gi = (bid >> 4) * 8 + (bid & 7);
  const int l15 = lane & 15, g4 = lane >> 4;

  if (wave == 0) {
    const int cw = 0; (void)cw;
    Loader ld{L, (u32)(size_t)smem, lane, -1};
    STAMP(16);
    const rsrc_t rwm = mkrs(p.wm, 1024u * 6144u * 2u);
    if (!isY) {
      const int x = gi;
      const u32 kvo = (u32)((x & 7) * 32768 + 28672 + 256 * (x >> 3)) * 256u;
      const rsrc_t rk = mkrs(p.kc, 8u * 32768u * 256u), rv = mkrs(p.vc, 8u * 32768u * 256u);
      ld.seg<256, false>(rk, 256, kvo, 0, 16, -1);
      bar();
      ld.seg<256>(rk, 256, kvo + 64 * 256, 16384, 48, -1);
      STAMP(17);
      ld.seg<256>(rv, 256, kvo, 65536, 64, -1);
      ld.drain();
      STAMP(18);
      bar();
      bar();
      STAMP(11);
      bar();
      bar();
      STAMP(12);
      STAMP(19);
      const u32 mo = (u32)(128 * (x >> 4)) * 12288u + (u32)(x & 15) * 128u;
      ld.seg<128>(rwm, 12288, mo + 3072 * 2, 0, 16, F_SB);
      ld.seg<128>(rwm, 12288, mo + 4096 * 2, 16384, 16, F_CB);
      if (x < 112) {
        const rsrc_t r1 = mkrs(p.w1, 1024u * 3584u * 2u);
        ld.seg<128>(r1, 7168, (u32)(512 * (x & 1)) * 7168u + (u32)(x >> 1) * 128u, 65536, 64, -1);
        ld.drain();
        STAMP(20);
        bar();
        bar();
        bar();
        bar();
      STAMP(13);
      } else ld.drain();
    } else {
      const int y = gi;
      const u32 mco = (u32)(256 * (y >> 5)) * 12288u + (u32)(y & 31) * 128u;
      ld.seg<128, false>(rwm, 12288, mco, 65536, 16, -1);
      bar();
      ld.seg<128>(rwm, 12288, mco + 128 * 12288, 65536 + 16384, 16, F_MAC);
      const rsrc_t rq = mkrs(p.wq, 1024u * 4096u * 2u);
      ld.seg<256>(rq, 8192, (u32)(256 * ((y & 7) >> 1)) * 8192u + (u32)((y >> 3) * 2 + (y & 1)) * 256u, 0, 64, -1);
      ld.drain();
      STAMP(17);
      bar();
      __builtin_amdgcn_s_sleep(2);
      const u32 mo = (u32)(128 * (y >> 4)) * 12288u + (u32)(y & 15) * 128u;
      ld.seg<128>(rwm, 12288, mo + 2048 * 2, L_GT, 16, F_GA);
      const rsrc_t ro = mkrs(p.wo, 4096u * 1024u * 2u);
      const u32 oo = (u32)(128 * ((y & 7) + 8 * (y >> 5))) * 2048u + (u32)((y >> 3) & 3) * 512u;
      lds_wait_ge(L, C_GATE + G_MAC, 4u);
      ld.seg<256>(ro, 2048, oo, 65536, 32, -1);
      ld.seg<256>(ro, 2048, oo + 256, 65536 + 32768, 32, -1);
      ld.drain();
      STAMP(18);
      bar();
      bar();
      STAMP(14);
      if (y < 64) lds_wait_ge(L, C_GATE + G_R, 2u);
      STAMP(19);
      lds_wait_ge(L, C_GATE + G_GA, 4u);
      ld.seg<128>(rwm, 12288, mo + 5120 * 2, L_GT, 16, F_GB);
      if (y < 112) {
        const rsrc_t r2 = mkrs(p.w2, 3584u * 1024u * 2u);
        const u32 o2 = (u32)(128 * (y >> 2)) * 2048u + (u32)(y & 3) * 512u;
        ld.seg<256>(r2, 2048, o2, 0, 32, -1);
        ld.seg<256>(r2, 2048, o2 + 256, 32768, 32, -1);
        ld.drain();
        STAMP(20);
        bar();
        bar();
      } else ld.drain();
    }
    return;
  }

  const int cw = wave - 1, ct = tid - 64;
  const rsrc_t rs = mkrs(p.ws, WS_END);
  if (ct < 64) *(LAS u32*)(L + L_CTL + 4 * ct) = 0u;
  const u64 epoch = __hip_atomic_load((GAS u64*)(p.ws + WS_EPOCH), __ATOMIC_RELAXED, __HIP_MEMORY_SCOPE_AGENT) + 1ull;
  const v2u cin = *(const GAS v2u*)(p.c + (ct >> 8) * 1024 + (ct & 255) * 4);
  const bool pl = l15 < 2;
  const int pub = l15 * 64 + g4 * 16;
  const int xfl = (l15 & 1) * XF_P + g4 * 16;
  lds_t cf_lane = L + L_CF + (l15 & 1) * CF_P + g4 * 16;
  auto silu_to_lds = [&]() {
    *(LAS v2u*)(L + L_CF + (ct >> 8) * CF_P + (ct & 255) * 8) = v2u{pk2(silu_f(bflo(cin.x)), silu_f(bfhi(cin.x))), pk2(silu_f(bflo(cin.y)), silu_f(bfhi(cin.y)))};
  };
  auto raise_flag = [&]() {
    wait_vm<0>();
    u32 old = 0;
    if (lane == 0) old = __hip_atomic_fetch_add((LAS u32*)(L + L_CTL + 4 * C_CLR), 1u, __ATOMIC_RELAXED, __HIP_MEMORY_SCOPE_WORKGROUP);
    if (__builtin_amdgcn_readfirstlane(old) == 7u && lane == 0) __hip_atomic_store((GAS u64*)(p.ws + WS_READY) + bid, epoch, __ATOMIC_RELAXED, __HIP_MEMORY_SCOPE_AGENT);
  };

  if (!isY) {
    const int x = gi, g = x & 7, rec = g * 16 + (x >> 3);
    int o_o3 = WS_O3 + rec * 2048 + ((cw & 1) * 64 + lane) * 16;
    int o_ml = WS_ML + rec * 128 + (lane & 7) * 8;
    int o_sc = WS_PG + (((1 + (cw >> 2)) * 64 + 4 * (x & 15) + (cw & 3)) * 8 + (x >> 4)) * 128 + pub;
    int o_p6 = WS_P6 + ((x >> 1) * 4 + (x & 1) * 2 + (cw >> 2)) * 512 + l15 * 256 + (cw & 3) * 64 + g4 * 16;
    if (cw < 2) st16(rs, o_o3, E4);
    if (cw == 2 && lane < 8) st8(rs, o_ml, E2);
    if (pl) st16(rs, o_sc, E4);
    if (x < 112 && pl) st16(rs, o_p6, E4);
    silu_to_lds();
    bar();
    raise_flag();
    int qoff[2];
    {
      const int base = WS_P2 + (((g >> 2) * 32 + 8 * (g & 3) + cw) * 4) * 512 + (lane & 31) * 16;
      qoff[0] = base + (lane >> 5) * 512; qoff[1] = base + ((lane >> 5) + 2) * 512;
    }
    lds_t kb = L + 256 * (32 * cw + l15) + 16 * (g4 ^ (l15 >> 2));
    lds_t qw = L + L_QF + cw * QF_P + (lane & 31) * 8 + (lane >> 5) * 4;
    lds_t qb = L + L_QF + (l15 & 7) * QF_P + g4 * 16;
    lds_t vb = L + 65536 + 256 * (4 * g4 + (l15 >> 2)) + 16 * ((2 * cw + ((lane & 3) >> 1)) ^ (((l15 >> 2) << 2) | g4)) + 8 * (lane & 1);
    lds_t pfw = L + L_PF + cw * 1024 + lane * 16, pfr = L + L_PF + lane * 16;
    lds_t mww = L + L_MW + l15 * 32 + cw * 4, mwr = L + L_MW + l15 * 32;
    lds_t otw = L + L_OT + (l15 & 7) * QF_P + (16 * cw + 4 * g4) * 2, otr = L + L_OT + (((cw & 1) * 64 + lane) >> 4) * QF_P + l15 * 16;
    PIN(qoff[0]); PIN(qoff[1]); PIN(kb); PIN(qw); PIN(qb); PIN(vb); PIN(pfw); PIN(pfr); PIN(mww); PIN(mwr); PIN(otw); PIN(otr); PIN(o_o3); PIN(o_ml);
    if (cw == 7) { __builtin_amdgcn_s_sleep(64); wait_ready(rs, lane, epoch); lds_set(L, C_RDY); }
    lds_wait_ge(L, C_RDY, 1u);
    bar();
    STAMP(1);
    {
      v4u kf[8], vf[8];
#pragma unroll
      for (int s = 0; s < 4; ++s) { const int ko = 64 * (s ^ (l15 & 3)); kf[s] = *(LAS v4u*)(kb + ko); kf[4 + s] = *(LAS v4u*)(kb + ko + 4096); }
#pragma unroll
      for (int w = 0; w < 8; ++w) vf[w] = tr2(vb + w * 8192, vb + w * 8192 + 4096);
      v4u v[2];
      gather16<2>(rs, qoff, v);
      STAMP(2);
      {
        const f4 a = u2f(v[0]) + u2f(v[1]);
        *(LAS u32*)qw = pk2(sw32(a.x, a.z), sw32(a.y, a.w));
      }
      bar();
      STAMP(11);
      f4 d0 = {0.f, 0.f, 0.f, 0.f}, d1 = {0.f, 0.f, 0.f, 0.f};
#pragma unroll
      for (int s = 0; s < 4; ++s) {
        const v4u qv = *(LAS v4u*)(qb + s * 64);
        d0 = mfma(kf[s], qv, d0);
        d1 = mfma(kf[4 + s], qv, d1);
      }
      float sc[8];
#pragma unroll
      for (int j = 0; j < 2; ++j) {
        const u32 a = pk2(d0[2 * j], d0[2 * j + 1]), b = pk2(d1[2 * j], d1[2 * j + 1]);
        const u32 c = pk2(bflo(a) * 0.08838834764831845f, bfhi(a) * 0.08838834764831845f), d = pk2(bflo(b) * 0.08838834764831845f, bfhi(b) * 0.08838834764831845f);
        sc[2 * j] = bflo(c); sc[2 * j + 1] = bfhi(c); sc[4 + 2 * j] = bflo(d); sc[5 + 2 * j] = bfhi(d);
      }
      float m = fmaxf(fmaxf(fmaxf(sc[0], sc[1]), fmaxf(sc[2], sc[3])), fmaxf(fmaxf(sc[4], sc[5]), fmaxf(sc[6], sc[7])));
      m = mx32(mx16(m));
#pragma unroll
      for (int j = 0; j < 8; ++j) sc[j] = __builtin_amdgcn_exp2f((sc[j] - m) * LOG2E);
      *(LAS v4u*)pfw = v4u{pk2(sc[0], sc[1]), pk2(sc[2], sc[3]), pk2(sc[4], sc[5]), pk2(sc[6], sc[7])};
      {
        float l = ((sc[0] + sc[1]) + (sc[2] + sc[3])) + ((sc[4] + sc[5]) + (sc[6] + sc[7]));
        l = sw16(l, l); l = sw32(l, l);
        if (g4 == 0) { *(LAS float*)mww = m; *(LAS float*)(mww + 512) = l; }
      }
      bar();
      STAMP(3);
      const f4 m0 = *(LAS f4*)mwr, m1 = *(LAS f4*)(mwr + 16);
      f4 d[8];
#pragma unroll
      for (int w = 0; w < 8; ++w) { const f4 z = {0.f, 0.f, 0.f, 0.f}; d[w] = mfma(vf[w], *(LAS v4u*)(pfr + w * 1024), z); }
      m = fmaxf(fmaxf(fmaxf(m0.x, m0.y), fmaxf(m0.z, m0.w)), fmaxf(fmaxf(m1.x, m1.y), fmaxf(m1.z, m1.w)));
      float f[8];
#pragma unroll
      for (int j = 0; j < 4; ++j) { f[j] = __builtin_amdgcn_exp2f((m0[j] - m) * LOG2E); f[4 + j] = __builtin_amdgcn_exp2f((m1[j] - m) * LOG2E); }
      const f4 acc = (((f[0] * d[0] + f[1] * d[1]) + (f[2] * d[2] + f[3] * d[3])) + ((f[4] * d[4] + f[5] * d[5]) + (f[6] * d[6] + f[7] * d[7])));
      if (l15 < 8) *(LAS v2u*)otw = v2u{sane(pk2(acc.x, acc.y)), sane(pk2(acc.z, acc.w))};
      if (cw == 2) {
        const f4 l0 = *(LAS f4*)(mwr + 512), l1 = *(LAS f4*)(mwr + 528);
        const float l = ((f[0] * l0.x + f[1] * l0.y) + (f[2] * l0.z + f[3] * l0.w)) + ((f[4] * l1.x + f[5] * l1.y) + (f[6] * l1.z + f[7] * l1.w));
        if (lane < 8) st8(rs, o_ml, v2u{fsane(m), fsane(l)});
      }
      bar();
      STAMP(12);
      if (cw < 2) st16(rs, o_o3, *(LAS v4u*)otr);
    }
    STAMP(4);
    {
      int a0, a1;
      tr_base<128>(lane, cw & 3, a0, a1);
      v4u xv[4], w[4];
      load_x<4>(cf_lane + (x >> 4) * 256, xv);
      lds_wait_ge(L, C_FULL + F_SB + (cw >> 2), 1u);
      load_w<128, 4>(L + (cw >> 2) * 16384 + a0, L + (cw >> 2) * 16384 + a1, w);
      const f4 a = mma<4>(w, xv);
      if (pl) st16(rs, o_sc, f2u(a));
    }
    STAMP(5);
    if (x < 112) {
      const int b = cw >> 2, k = 256 * (cw & 3) + 4 * lane;
      const int ro = (16 * (cw & 3) + (lane >> 2)) * 128 + b * 32 + (lane & 3) * 8;
      int offs[2] = {WS_SC + ro, WS_SC + ro + 64}, offx[1] = {WS_X1 + ro};
      int a0, a1;
      tr_base<128>(lane, cw & 3, a0, a1);
      lds_t wa0 = L + 65536 + (cw >> 2) * 32768 + a0, wa1 = L + 65536 + (cw >> 2) * 32768 + a1;
      lds_t xr = L + L_XF + xfl + (cw >> 2) * 512, xw = L + L_XF + b * XF_P + (k & 511) * 2;
      lds_t redw = L + L_RED + cw * 8 + (g4 & 1) * 4, redr = L + L_RED + b * 32, x1w = L + L_X1 + b * 2048 + k * 2;
      PIN(offs[0]); PIN(offs[1]); PIN(offx[0]); PIN(wa0); PIN(wa1); PIN(xr); PIN(xw); PIN(redw); PIN(redr); PIN(x1w); PIN(o_p6);
      bar();
      STAMP(6);
      v4u w[8];
      load_w<128, 8>(wa0, wa1, w);
      v2u vs[2], vx[1];
      gather8<2>(rs, offs, vs);
      const float sb[4] = {bflo(vs[0].x), bfhi(vs[0].x), bflo(vs[0].y), bfhi(vs[0].y)};
      const u32 c01 = pk2(1.f + bflo(vs[1].x), 1.f + bfhi(vs[1].x)), c23 = pk2(1.f + bflo(vs[1].y), 1.f + bfhi(vs[1].y));
      const float cb[4] = {bflo(c01), bfhi(c01), bflo(c23), bfhi(c23)};
      gather8<1>(rs, offx, vx);
      STAMP(7);
      float e[4] = {bflo(vx[0].x), bfhi(vx[0].x), bflo(vx[0].y), bfhi(vx[0].y)};
#pragma unroll
      for (int pass = 0; pass < 2; ++pass) {
        float s1 = row_sum16((e[0] + e[1]) + (e[2] + e[3])), s2 = row_sum16((e[0] * e[0] + e[1] * e[1]) + (e[2] * e[2] + e[3] * e[3]));
        float s = sw16(s1, s2);
        s = sw32(s, s);
        *(LAS float*)(redw + pass * 64) = s;
        if (pass == 0) *(LAS v2u*)x1w = vx[0];
        bar();
        const f4 r0 = *(LAS f4*)(redr + pass * 64), r1 = *(LAS f4*)(redr + pass * 64 + 16);
        const float mean = ((r0.x + r0.z) + (r1.x + r1.z)) * (1.f / 1024.f);
        const float var = ((r0.y + r0.w) + (r1.y + r1.w)) * (1.f / 1024.f) - mean * mean;
        float m, rden;
        ln_fin(mean, fmaxf(var, 0.f), m, rden);
        const u32 d01 = pk2(e[0] - m, e[1] - m), d23 = pk2(e[2] - m, e[3] - m);
        u32 z01 = pk2(bflo(d01) * rden, bfhi(d01) * rden), z23 = pk2(bflo(d23) * rden, bfhi(d23) * rden);
        if (pass == 0) {
          const u32 t01 = pk2(bflo(z01) * cb[0], bfhi(z01) * cb[1]), t23 = pk2(bflo(z23) * cb[2], bfhi(z23) * cb[3]);
          z01 = pk2(bflo(t01) + sb[0], bfhi(t01) + sb[1]); z23 = pk2(bflo(t23) + sb[2], bfhi(t23) + sb[3]);
          e[0] = bflo(z01); e[1] = bfhi(z01); e[2] = bflo(z23); e[3] = bfhi(z23);
        } else if (((cw & 3) >> 1) == (x & 1)) *(LAS v2u*)xw = v2u{z01, z23};
      }
      bar();
      STAMP(13);
      v4u xv[8];
      load_x<8>(xr, xv);
      const f4 a = mma<8>(w, xv);
      if (pl) st16(rs, o_p6, f2u(a));
      STAMP(8);
    }
    if (x < 64 && cw == 0) {
      const int ch = lane & 7, hi = lane >> 3;
      const v2u b2v = *(const GAS v2u*)(p.b2 + 16 * x + 4 * (ch & 3));
      int offg[1] = {WS_PG + ((3 * 64 + x) * 8 + hi) * 128 + ch * 16};
      const int pb = WS_P7 + x * 28 * 128 + ch * 16;
      int off[4] = {pb + hi * 128, pb + (hi + 8) * 128, pb + (hi + 16) * 128, pb + (hi < 4 ? hi + 24 : 0) * 128};
      v4u vg[1];
      gather16<1>(rs, offg, vg);
      float gb = rows4(u2f(vg[0]));
      gb = rbf(gb + dpp<ROR8>(gb));
      const int col = 16 * x + 4 * (ch & 3) + g4;
      const float x1 = bflo(*(LAS u16*)(L + L_X1 + (ch >> 2) * 2048 + col * 2));
      const float b2 = g4 == 0 ? bflo(b2v.x) : g4 == 1 ? bfhi(b2v.x) : g4 == 2 ? bflo(b2v.y) : bfhi(b2v.y);
      GAS u16* op = (GAS u16*)p.out + (ch >> 2) * 1024 + col;
      v4u v[4];
      gather16<4>(rs, off, v);
      STAMP(9);
      f4 a = (u2f(v[0]) + u2f(v[1])) + u2f(v[2]);
      if (hi < 4) a += u2f(v[3]);
      float s = rows4(a);
      s += dpp<ROR8>(s);
      const float o = x1 + rbf(gb * rbf(rbf(s) + b2));
      if ((lane & 8) == 0) *op = (u16)pk2(o, o);
      if (x == 0 && lane == 0) __hip_atomic_store((GAS u64*)(p.ws + WS_EPOCH), epoch, __ATOMIC_RELAXED, __HIP_MEMORY_SCOPE_AGENT);
      STAMP(10);
    }
  } else {
    const int y = gi;
    const int qks = (y & 7) >> 1, qh = (y >> 3) * 2 + (y & 1);
    const int oh = (y & 7) + 8 * (y >> 5), ons = (y >> 3) & 3;
    int o_mac = WS_P1 + ((y & 31) * 4 + (y >> 5)) * 512 + l15 * 256 + (cw & 3) * 64 + g4 * 16;
    int o_p2 = WS_P2 + ((l15 * 32 + qh) * 4 + qks) * 512 + cw * 64 + g4 * 16;
    int o_g = WS_PG + ((4 * (y & 15) + (cw & 3)) * 8 + (y >> 4)) * 128 + pub;
    const int rblk = (cw >> 2) * 8 + 2 * (cw & 3);
    int o_p4 = WS_P4 + ((ons * 16 + rblk) * 32 + oh) * 128 + pub;
    int o_p7 = WS_P7 + (((y & 3) * 16 + rblk) * 28 + (y >> 2)) * 128 + pub;
    int o_r = (cw == 0 ? WS_X1 : WS_SC) + y * 128 + lane * 8;
    if (pl) {
      if (cw < 4) st16(rs, o_mac, E4);
      else { st16(rs, o_g, E4); st16(rs, o_g + 3 * 64 * 8 * 128, E4); }
      st16(rs, o_p2, E4);
      st16(rs, o_p4, E4); st16(rs, o_p4 + 32 * 128, E4);
      if (y < 112) { st16(rs, o_p7, E4); st16(rs, o_p7 + 28 * 128, E4); }
    }
    if (y < 64 && ((cw == 0 && lane < 8) || (cw == 1 && lane < 16))) st8(rs, o_r, E2);
    const GAS v4u* xg = (const GAS v4u*)p.x;
    const v4u xa0 = xg[lane * 2], xa1 = xg[lane * 2 + 1], xb0 = xg[128 + lane * 2], xb1 = xg[128 + lane * 2 + 1];
    const int hb = (lane >> 3) & 1, hk = 32 * cw + 4 * (lane & 7) + g4;
    const u32 xq = ((const GAS u16*)p.x)[hb * 1024 + 256 * qks + hk];
    silu_to_lds();
    bar();
    raise_flag();
    if (cw < 4) {
      int a0, a1;
      tr_base<128>(lane, cw, a0, a1);
      v4u xv[8], w[8];
      load_x<8>(cf_lane + (y >> 5) * 512, xv);
      lds_t wa0 = L + 65536 + a0, wa1 = L + 65536 + a1;
      PIN(wa0); PIN(wa1); PIN(o_mac);
      lds_wait_ge(L, C_FULL + F_MAC, 1u);
      STAMP(1);
      load_w<128, 8>(wa0, wa1, w);
      const f4 a = mma<8>(w, xv);
      if (pl) st16(rs, o_mac, f2u(a));
      lds_add(L, C_GATE + G_MAC, lane);
      STAMP(2);
    }
    float ln;
    {
      float m[2], rd[2];
#pragma unroll
      for (int b = 0; b < 2; ++b) {
        const v4u u0 = b ? xb0 : xa0, u1 = b ? xb1 : xa1;
        float e[16];
#pragma unroll
        for (int j = 0; j < 4; ++j) { e[2 * j] = bflo(u0[j]); e[2 * j + 1] = bfhi(u0[j]); e[8 + 2 * j] = bflo(u1[j]); e[9 + 2 * j] = bfhi(u1[j]); }
        float s = 0.f;
#pragma unroll
        for (int j = 0; j < 16; ++j) s += e[j];
        s = row_sum16(s); s = sw16(s, s); s = sw32(s, s);
        const float mean = s * (1.f / 1024.f);
        float s2 = 0.f;
#pragma unroll
        for (int j = 0; j < 16; ++j) { const float d = e[j] - mean; s2 += d * d; }
        s2 = row_sum16(s2); s2 = sw16(s2, s2); s2 = sw32(s2, s2);
        ln_fin(mean, s2 * (1.f / 1024.f), m[b], rd[b]);
      }
      ln = rbf(rbf(bflo(xq) - (hb ? m[1] : m[0])) * (hb ? rd[1] : rd[0]));
    }
    {
      int off[2];
      off[0] = WS_P1 + ((4 * qks + (cw >> 1)) * 4 + g4) * 512 + hb * 256 + (32 * (cw & 1) + 4 * (lane & 7)) * 4;
      off[1] = off[0] + 16 * 4 * 512;
      int a0, a1;
      tr_base<256>(lane, cw, a0, a1);
      lds_t wa0 = L + a0, wa1 = L + a1, xw = L + L_XF + hb * XF_P + hk * 2, xr = L + L_XF + xfl;
      PIN(ln); PIN(off[0]); PIN(off[1]); PIN(wa0); PIN(wa1); PIN(xw); PIN(xr); PIN(o_p2);
      if (cw == 7) { wait_ready(rs, lane, epoch); lds_set(L, C_RDY); }
      lds_wait_ge(L, C_RDY, 1u);
      bar();
      STAMP(3);
      v4u v[2], w[8];
      v[0] = ld16(rs, off[0]); v[1] = ld16(rs, off[1]);
      SCHED();
      load_w<256, 8>(wa0, wa1, w);
      if (!wave_full(umax(mx4(v[0]), mx4(v[1])))) gather16<2>(rs, off, v);
      STAMP(4);
      const u32 sc = pk2(rows4(u2f(v[0])), rows4(u2f(v[1])));
      const float h = rbf(ln * rbf(1.f + bfhi(sc))) + bflo(sc);
      *(LAS u16*)xw = (u16)pk2(h, h);
      flag_sync(L, cw, lane);
      STAMP(15);
      v4u xv[8];
      load_x<8>(xr, xv);
      const f4 a = mma<8>(w, xv);
      if (pl) st16(rs, o_p2, f2u(a));
      STAMP(5);
    }
    int ga0, ga1;
    tr_base<128>(lane, cw & 3, ga0, ga1);
    if (cw >= 4) {
      v4u xv[4], w[4];
      load_x<4>(cf_lane + (y >> 4) * 256, xv);
      lds_wait_ge(L, C_FULL + F_GA, 1u);
      load_w<128, 4>(L + L_GT + ga0, L + L_GT + ga1, w);
      const f4 a = mma<4>(w, xv);
      if (pl) st16(rs, o_g, f2u(a));
      lds_add(L, C_GATE + G_GA, lane);
    }
    int ta0, ta1, tb0, tb1;
    tr_base<256>(lane, 2 * (cw & 3), ta0, ta1);
    tr_base<256>(lane, 2 * (cw & 3) + 1, tb0, tb1);
    {
      const int b = (cw >> 1) & 1, dh = cw & 1, g = b * 4 + (oh >> 3), hh = oh & 7, s0 = lane >> 3;
      int ob = WS_O3 + ((g * 16 + s0) * 8 + hh) * 256 + dh * 128 + (lane & 7) * 16;
      int mb = WS_ML + (g * 16 + s0) * 128 + hh * 8;
      lds_t tile = L + 65536 + (cw >> 2) * 32768;
      lds_t pa0 = tile + ta0, pa1 = tile + ta1, pb0 = tile + tb0, pb1 = tile + tb1;
      lds_t xw = L + L_XF + 2 * XF_P + b * XF_P + (64 * dh + 8 * (lane & 7) + 2 * g4) * 2, xr = L + L_XF + 2 * XF_P + xfl;
      PIN(ob); PIN(mb); PIN(pa0); PIN(pa1); PIN(pb0); PIN(pb1); PIN(xw); PIN(xr); PIN(o_p4);
      bar();
      STAMP(6);
      v4u wa[4], wb[4];
      load_w<256, 4>(pa0, pa1, wa);
      load_w<256, 4>(pb0, pb1, wb);
      if (cw < 4) {
        v4u v[2]; v2u ml[2];
#pragma unroll 1
        for (u32 sp = 0; sp < SPIN_G; ++sp) {
          ml[0] = ld8(rs, mb); ml[1] = ld8(rs, mb + 8 * 128);
          v[0] = ld16(rs, ob); v[1] = ld16(rs, ob + 8 * 2048);
          if (wave_full(umax(umax(mx2(ml[0]), mx2(ml[1])), umax(mx4(v[0]), mx4(v[1]))))) break;
          __builtin_amdgcn_s_sleep(MK_RSLEEP);
        }
        STAMP(7);
        const float m0 = asf(ml[0].x), m1 = asf(ml[1].x);
        float M = fmaxf(m0, m1);
        M = fmaxf(M, dpp<ROR8>(M));
        M = mx32(mx16(M));
        const float e0 = __builtin_amdgcn_exp2f((m0 - M) * LOG2E), e1 = __builtin_amdgcn_exp2f((m1 - M) * LOG2E);
        float Ls = e0 * asf(ml[0].y) + e1 * asf(ml[1].y);
        Ls += dpp<ROR8>(Ls);
        Ls = sw16(Ls, Ls); Ls = sw32(Ls, Ls);
        float o[8];
#pragma unroll
        for (int j = 0; j < 4; ++j) {
          o[2 * j] = e0 * bflo(v[0][j]) + e1 * bflo(v[1][j]);
          o[2 * j + 1] = e0 * bfhi(v[0][j]) + e1 * bfhi(v[1][j]);
        }
        float t0 = sw16(sw32(o[0], o[4]), sw32(o[2], o[6])), t1 = sw16(sw32(o[1], o[5]), sw32(o[3], o[7]));
        t0 += dpp<ROR8>(t0); t1 += dpp<ROR8>(t1);
        const float inv = __builtin_amdgcn_rcpf(Ls);
        if ((lane & 8) == 0) *(LAS u32*)xw = pk2(t0 * inv, t1 * inv);
      }
      bar();
      STAMP(14);
      v4u xv[4];
      load_x<4>(xr, xv);
      const f4 a0 = mma<4>(wa, xv), a1 = mma<4>(wb, xv);
      if (pl) { st16(rs, o_p4, f2u(a0)); st16(rs, o_p4 + 32 * 128, f2u(a1)); }
      STAMP(8);
    }
    if (y < 64 && cw < 2) {
      const int ch = lane & 7, hi = lane >> 3;
      if (cw == 0) {
        const u32 xv = *(const GAS u32*)(p.x + (ch >> 2) * 1024 + 16 * y + 4 * (ch & 3) + 2 * (lane >> 5));
        int offg[1] = {WS_PG + ((0 * 64 + y) * 8 + hi) * 128 + ch * 16};
        const int pb = WS_P4 + (y * 32 + hi) * 128 + ch * 16;
        int off[4] = {pb, pb + 8 * 128, pb + 16 * 128, pb + 24 * 128};
        int ost = WS_X1 + y * 128 + ch * 8 + (lane >> 5) * 4;
        v4u vg[1];
        gather16<1>(rs, offg, vg);
        const f4 gg = u2f(vg[0]);
        float g0 = sw32(gg.x, gg.z), g1 = sw32(gg.y, gg.w);
        g0 = sw16(g0, g0); g1 = sw16(g1, g1);
        g0 = rbf(g0 + dpp<ROR8>(g0)); g1 = rbf(g1 + dpp<ROR8>(g1));
        PIN(g0); PIN(g1); PIN(ost);
        v4u v[4];
        gather16<4>(rs, off, v);
        STAMP(9);
        const f4 a = (u2f(v[0]) + u2f(v[1])) + (u2f(v[2]) + u2f(v[3]));
        float s0 = sw32(a.x, a.z), s1 = sw32(a.y, a.w);
        s0 = sw16(s0, s0); s1 = sw16(s1, s1);
        const u32 at = pk2(s0 + dpp<ROR8>(s0), s1 + dpp<ROR8>(s1));
        const u32 t = pk2(g0 * bflo(at), g1 * bfhi(at));
        const u32 o = sane(pk2(bflo(xv) + bflo(t), bfhi(xv) + bfhi(t)));
        if ((lane & 24) == 0) __builtin_amdgcn_raw_buffer_store_b32(o, rs, ost, 0, SC1);
        STAMP(10);
      } else {
        int off[2] = {WS_PG + ((1 * 64 + y) * 8 + hi) * 128 + ch * 16, WS_PG + ((2 * 64 + y) * 8 + hi) * 128 + ch * 16};
        v4u v[2];
        gather16<2>(rs, off, v);
        const f4 a = u2f(v[0]), c = u2f(v[1]);
        float s0 = sw16(sw32(a.x, a.z), sw32(c.x, c.z)), s1 = sw16(sw32(a.y, a.w), sw32(c.y, c.w));
        s0 += dpp<ROR8>(s0); s1 += dpp<ROR8>(s1);
        const int ost = WS_SC + y * 128 + (g4 & 1) * 64 + ch * 8 + (lane >> 5) * 4;
        if ((lane & 8) == 0) __builtin_amdgcn_raw_buffer_store_b32(sane(pk2(s0, s1)), rs, ost, 0, SC1);
      }
      lds_add(L, C_GATE + G_R, lane);
    }
    if (cw >= 4) {
      v4u xv[4], w[4];
      load_x<4>(cf_lane + (y >> 4) * 256, xv);
      lds_wait_ge(L, C_FULL + F_GB, 1u);
      load_w<128, 4>(L + L_GT + ga0, L + L_GT + ga1, w);
      const f4 a = mma<4>(w, xv);
      if (pl) st16(rs, o_g + 3 * 64 * 8 * 128, f2u(a));
    }
    if (y < 112) {
      const int fs = y >> 2;
      const int id = 16 * (cw & 3) + l15, nsl = id >> 5, b = (id >> 4) & 1, quad = id & 15;
      float b1v = bflo(((const GAS u16*)p.b1)[128 * fs + 64 * nsl + 4 * quad + g4]);
      int off[1] = {WS_P6 + ((2 * fs + nsl) * 4 + g4) * 512 + b * 256 + quad * 16};
      lds_t tile = L + (cw >> 2) * 32768;
      lds_t pa0 = tile + ta0, pa1 = tile + ta1, pb0 = tile + tb0, pb1 = tile + tb1;
      lds_t xw = L + L_XF + 4 * XF_P + b * XF_P + (64 * nsl + 4 * quad + g4) * 2, xr = L + L_XF + 4 * XF_P + xfl;
      PIN(b1v); PIN(off[0]); PIN(pa0); PIN(pa1); PIN(pb0); PIN(pb1); PIN(xw); PIN(xr); PIN(o_p7);
      bar();
      STAMP(11);
      v4u wa[4], wb[4];
      load_w<256, 4>(pa0, pa1, wa);
      load_w<256, 4>(pb0, pb1, wb);
      if (cw < 4) {
        v4u v[1];
        gather16<1>(rs, off, v);
        STAMP(12);
        const float hv = gelu_f(rbf(rbf(rows4(u2f(v[0]))) + b1v));
        *(LAS u16*)xw = (u16)pk2(hv, hv);
      }
      bar();
      v4u xv[4];
      load_x<4>(xr, xv);
      const f4 a0 = mma<4>(wa, xv), a1 = mma<4>(wb, xv);
      if (pl) { st16(rs, o_p7, f2u(a0)); st16(rs, o_p7 + 28 * 128, f2u(a1)); }
      STAMP(13);
    }
  }
  if (gi == 0 && isY == 0 && cw == 0) {
    if (lane == 0) __hip_atomic_store((GAS u64*)(p.ws + (6 << 20)), (u64)__builtin_amdgcn_s_memrealtime(), __ATOMIC_RELAXED, __HIP_MEMORY_SCOPE_AGENT);
  }
}
}

extern "C" void kernel_launch(void* const* d_in, const int* in_sizes, int n_in, void* d_out, int out_size, void* d_ws, size_t ws_size, hipStream_t stream) {
  using namespace mk;
  Params p;
  p.x = (const u16*)d_in[0]; p.kc = (const u16*)d_in[1]; p.vc = (const u16*)d_in[2]; p.wq = (const u16*)d_in[3]; p.wo = (const u16*)d_in[4];
  p.w1 = (const u16*)d_in[6]; p.b1 = (const u16*)d_in[7]; p.w2 = (const u16*)d_in[8]; p.b2 = (const u16*)d_in[9];
  p.c = (const u16*)d_in[10]; p.wm = (const u16*)d_in[11];
  p.out = (u16*)d_out; p.ws = (unsigned char*)d_ws;
  (void)hipFuncSetAttribute((const void*)mega, hipFuncAttributeMaxDynamicSharedMemorySize, LDS_BYTES);
  hipLaunchKernelGGL(mega, dim3(NWG), dim3(NTHR), LDS_BYTES, stream, p);
  hipLaunchKernelGGL(post_k, dim3(1), dim3(64), 0, stream, p.ws, POSTK - 1);
}
```

```cpp
#define DGRID 256
#define DTHR 64
#define DLDS 0
#define DVGPR 0
#define DBLK 0
#include <hip/hip_runtime.h>
#include <stdint.h>
#pragma clang fp contract(off)

namespace mk {
#define GAS __attribute__((address_space(1)))
#define LAS __attribute__((address_space(3)))
typedef unsigned short u16;
typedef unsigned u32;
typedef unsigned long long u64;
typedef u32 v4u __attribute__((ext_vector_type(4)));
typedef u32 v2u __attribute__((ext_vector_type(2)));
typedef float f4 __attribute__((ext_vector_type(4)));
typedef float f2 __attribute__((ext_vector_type(2)));
typedef short s4 __attribute__((ext_vector_type(4)));
typedef __bf16 bf8 __attribute__((ext_vector_type(8)));
typedef __bf16 bf2 __attribute__((ext_vector_type(2)));
typedef LAS unsigned char* lds_t;

constexpr int NWG = 256, NTHR = 576;
constexpr u32 EMPTY = 0xFFFFFFFFu;
constexpr int RSRC_W3 = 0x00020000;
constexpr int SC1 = 16;
constexpr u32 SPIN_G = 1u << 17;
constexpr u32 SPIN_L = 1u << 22;
constexpr float LOG2E = 1.4426950408889634f;

constexpr int WS_EPOCH = 0, WS_READY = 256;
constexpr int WS_P1 = 4096;
constexpr int WS_P2 = WS_P1 + 128 * 512;
constexpr int WS_O3 = WS_P2 + 256 * 512;
constexpr int WS_ML = WS_O3 + 128 * 2048;
constexpr int WS_P4 = WS_ML + 128 * 128;
constexpr int WS_PG = WS_P4 + 64 * 32 * 128;
constexpr int WS_X1 = WS_PG + 4 * 64 * 8 * 128;
constexpr int WS_SC = WS_X1 + 64 * 128;
constexpr int WS_P6 = WS_SC + 64 * 128;
constexpr int WS_P7 = WS_P6 + 56 * 2048;
constexpr int WS_END = WS_P7 + 64 * 28 * 128;
constexpr int WS_STAMP = 4 << 20;

constexpr int L_CTL = 131072;
constexpr int L_STAMP = L_CTL + 256;
constexpr int L_CF = L_STAMP + 1280;
constexpr int CF_P = 2048 + 16;
constexpr int L_XF = L_CF + 2 * CF_P;
constexpr int XF_P = 1024 + 16;
constexpr int L_G = L_XF + 6 * XF_P;
constexpr int QF_P = 256 + 16;
constexpr int L_QF = L_G;
constexpr int L_PF = L_QF + 8 * QF_P;
constexpr int L_MW = L_PF + 8192;
constexpr int L_OT = L_MW + 1024;
constexpr int L_RED = L_OT + 8 * QF_P;
constexpr int L_X1 = L_RED + 256;
constexpr int L_XEND = L_X1 + 4096;
constexpr int L_GT = L_G;
constexpr int L_YEND = L_GT + 16384;
constexpr int LDS_BYTES = 160 * 1024;
static_assert(L_XEND <= LDS_BYTES && L_YEND <= LDS_BYTES, "lds");
static_assert((L_CF % 16) == 0 && (L_XF % 16) == 0 && (L_G % 16) == 0 && (L_PF % 16) == 0 && (L_MW % 16) == 0 && (L_OT % 16) == 0 && (L_RED % 16) == 0 && (L_X1 % 16) == 0, "align");
enum { C_FULL = 0  , C_FLG = 8  , C_GATE = 16  , C_CLR = 24, C_RDY = 25, C_DONE = 26 };
#define MK_NINF 16
constexpr int NINF = MK_NINF;

struct Params {
  const u16 *x, *kc, *vc, *wq, *wo, *w1, *b1, *w2, *b2, *c, *wm;
  u16* out;
  unsigned char* ws;
};

#define PIN(x) asm volatile("" : "+v"(x))
#define SCHED() __builtin_amdgcn_sched_barrier(0)
__device__ __forceinline__ float asf(u32 b) { return __builtin_bit_cast(float, b); }
__device__ __forceinline__ u32 asu(float f) { return __builtin_bit_cast(u32, f); }
__device__ __forceinline__ float bflo(u32 w) { return asf(w << 16); }
__device__ __forceinline__ float bfhi(u32 w) { return asf(w & 0xffff0000u); }
__device__ __forceinline__ u32 pk2(float lo, float hi) { bf2 v = {(__bf16)lo, (__bf16)hi}; return __builtin_bit_cast(u32, v); }
__device__ __forceinline__ float rbf(float f) { return bflo(pk2(f, f)); }
__device__ __forceinline__ u32 umin(u32 a, u32 b) { return a < b ? a : b; }
__device__ __forceinline__ u32 umax(u32 a, u32 b) { return a > b ? a : b; }
__device__ __forceinline__ u32 sane(u32 w) { return umin(w, 0xFFFEFFFFu); }
__device__ __forceinline__ u32 fsane(float f) { return umin(asu(f), 0xFFFFFFFEu); }
template <int CTRL> __device__ __forceinline__ float dpp(float x) { return __builtin_bit_cast(float, __builtin_amdgcn_mov_dpp(__builtin_bit_cast(int, x), CTRL, 0xf, 0xf, true)); }
constexpr int XOR1 = 0xB1, XOR2 = 0x4E, XOR7 = 0x141, ROR8 = 0x128;
__device__ __forceinline__ float sw32(float a, float b) { auto s = __builtin_amdgcn_permlane32_swap(asu(a), asu(b), false, false); return asf((u32)s[0]) + asf((u32)s[1]); }
__device__ __forceinline__ float sw16(float a, float b) { auto s = __builtin_amdgcn_permlane16_swap(asu(a), asu(b), false, false); return asf((u32)s[0]) + asf((u32)s[1]); }
__device__ __forceinline__ float mx32(float a) { auto s = __builtin_amdgcn_permlane32_swap(asu(a), asu(a), false, false); return fmaxf(asf((u32)s[0]), asf((u32)s[1])); }
__device__ __forceinline__ float mx16(float a) { auto s = __builtin_amdgcn_permlane16_swap(asu(a), asu(a), false, false); return fmaxf(asf((u32)s[0]), asf((u32)s[1])); }
__device__ __forceinline__ float rows4(f4 v) { return sw16(sw32(v.x, v.z), sw32(v.y, v.w)); }
__device__ __forceinline__ float row_sum16(float x) { x += dpp<XOR1>(x); x += dpp<XOR2>(x); x += dpp<XOR7>(x); x += dpp<ROR8>(x); return x; }
__device__ __forceinline__ f4 mfma(v4u a, v4u b, f4 c) { return __builtin_amdgcn_mfma_f32_16x16x32_bf16(__builtin_bit_cast(bf8, a), __builtin_bit_cast(bf8, b), c, 0, 0, 0); }
__device__ __forceinline__ v4u tr2(lds_t a0, lds_t a1) {
  s4 r0 = __builtin_amdgcn_ds_read_tr16_b64_v4i16((LAS s4*)a0), r1 = __builtin_amdgcn_ds_read_tr16_b64_v4i16((LAS s4*)a1);
  return __builtin_bit_cast(v4u, __builtin_shufflevector(r0, r1, 0, 1, 2, 3, 4, 5, 6, 7));
}
template <int N> __device__ __forceinline__ void wait_vm() { asm volatile("s_waitcnt vmcnt(%0)" ::"n"(N) : "memory"); }
__device__ __forceinline__ void wait_lgkm() { asm volatile("s_waitcnt lgkmcnt(0)" ::: "memory"); }
__device__ __forceinline__ void bar() { asm volatile("s_waitcnt lgkmcnt(0)\n\ts_barrier" ::: "memory"); }
__device__ __forceinline__ u32 lds_peek(lds_t L, int word) { return __builtin_amdgcn_readfirstlane(*(volatile LAS u32*)(L + L_CTL + 4 * word)); }
__device__ __forceinline__ void lds_wait_ge(lds_t L, int word, u32 want) {
#pragma unroll 1
  for (u32 sp = 0; sp < SPIN_L; ++sp) if (lds_peek(L, word) >= want) break;
  asm volatile("" ::: "memory");
}
__device__ __forceinline__ void lds_set(lds_t L, int word) { *(volatile LAS u32*)(L + L_CTL + 4 * word) = 1u; }
__device__ __forceinline__ void lds_add(lds_t L, int word, int lane) {
  if (lane == 0) (void)__hip_atomic_fetch_add((LAS u32*)(L + L_CTL + 4 * word), 1u, __ATOMIC_RELAXED, __HIP_MEMORY_SCOPE_WORKGROUP);
}
__device__ __forceinline__ void flag_sync(lds_t L, int cw, int lane) {
  *(volatile LAS u32*)(L + L_CTL + 4 * (C_FLG + cw)) = 1u;
#pragma unroll 1
  for (u32 sp = 0; sp < SPIN_L; ++sp) {
    const u32 v = *(volatile LAS u32*)(L + L_CTL + 4 * (C_FLG + (lane & 7)));
    if (__builtin_amdgcn_ballot_w64(v == 0u) == 0ull) break;
  }
  asm volatile("" ::: "memory");
}

#define STAMP(i) do {} while (0)

typedef __amdgpu_buffer_rsrc_t rsrc_t;
__device__ __forceinline__ rsrc_t mkrs(const void* p, u32 bytes) { return __builtin_amdgcn_make_buffer_rsrc((void*)p, 0, (int)bytes, RSRC_W3); }
__device__ __forceinline__ v4u ld16(rsrc_t rs, int off) { return __builtin_amdgcn_raw_buffer_load_b128(rs, off, 0, SC1); }
__device__ __forceinline__ v2u ld8(rsrc_t rs, int off) { return __builtin_amdgcn_raw_buffer_load_b64(rs, off, 0, SC1); }
__device__ __forceinline__ void st16(rsrc_t rs, int off, v4u v) { __builtin_amdgcn_raw_buffer_store_b128(v, rs, off, 0, SC1); }
__device__ __forceinline__ void st8(rsrc_t rs, int off, v2u v) { __builtin_amdgcn_raw_buffer_store_b64(v, rs, off, 0, SC1); }
__device__ __forceinline__ u32 mx4(v4u v) { return umax(umax(v.x, v.y), umax(v.z, v.w)); }
__device__ __forceinline__ u32 mx2(v2u v) { return umax(v.x, v.y); }
__device__ __forceinline__ bool wave_full(u32 m) { return __builtin_amdgcn_ballot_w64(m == EMPTY) == 0ull; }
__device__ __forceinline__ v4u f2u(f4 v) { return v4u{fsane(v.x), fsane(v.y), fsane(v.z), fsane(v.w)}; }
__device__ __forceinline__ f4 u2f(v4u v) { return __builtin_bit_cast(f4, v); }
constexpr v4u E4 = {EMPTY, EMPTY, EMPTY, EMPTY};
constexpr v2u E2 = {EMPTY, EMPTY};

#define MK_RSLEEP 4
template <int N> __device__ __forceinline__ void gather16(rsrc_t rs, const int (&off)[N], v4u (&v)[N]) {
#pragma unroll 1
  for (u32 sp = 0; sp < SPIN_G; ++sp) {
#pragma unroll
    for (int i = 0; i < N; ++i) v[i] = ld16(rs, off[i]);
    u32 m = mx4(v[0]);
#pragma unroll
    for (int i = 1; i < N; ++i) m = umax(m, mx4(v[i]));
    if (wave_full(m)) break;
    __builtin_amdgcn_s_sleep(MK_RSLEEP);
  }
}
template <int N> __device__ __forceinline__ void gather8(rsrc_t rs, const int (&off)[N], v2u (&v)[N]) {
#pragma unroll 1
  for (u32 sp = 0; sp < SPIN_G; ++sp) {
#pragma unroll
    for (int i = 0; i < N; ++i) v[i] = ld8(rs, off[i]);
    u32 m = mx2(v[0]);
#pragma unroll
    for (int i = 1; i < N; ++i) m = umax(m, mx2(v[i]));
    if (wave_full(m)) break;
    __builtin_amdgcn_s_sleep(MK_RSLEEP);
  }
}
__device__ __forceinline__ void wait_ready(rsrc_t rs, int lane, u64 ep) {
#pragma unroll 1
  for (u32 sp = 0; sp < SPIN_G; ++sp) {
    bool ok = true;
#pragma unroll
    for (int i = 0; i < 2; ++i) {
      const v4u v = ld16(rs, WS_READY + i * 1024 + lane * 16);
      ok &= (v.x == (u32)ep) & (v.y == (u32)(ep >> 32)) & (v.z == (u32)ep) & (v.w == (u32)(ep >> 32));
    }
    if (__builtin_amdgcn_ballot_w64(!ok) == 0ull) break;
    __builtin_amdgcn_s_sleep(8);
  }
}

__device__ __forceinline__ void dma1(u32 lds, int voff, rsrc_t rs, u32 soff) {
  asm volatile("s_mov_b32 m0, %0\n\ts_nop 0\n\tbuffer_load_dwordx4 %1, %2, %3 offen nt lds" ::"s"(lds), "v"(voff), "s"(rs), "s"(soff) : "memory");
}
struct Loader {
  lds_t L;
  u32 lbase;
  int lane, pend;
  template <int PITCH, bool PACE = true> __device__ __forceinline__ void seg(rsrc_t rs, u32 stride, u32 sbase, u32 ldst, int n, int flag) {
    int vo[4];
    if constexpr (PITCH == 256) {
#pragma unroll
      for (int j = 0; j < 4; ++j) vo[j] = (lane >> 4) * stride + 16 * ((lane & 15) ^ (((lane >> 4) << 2) | j));
    } else {
#pragma unroll
      for (int j = 0; j < 4; ++j) { const int f = ((lane >> 4) & 1) | ((j & 1) << 1); vo[j] = (lane >> 3) * stride + 32 * (((lane & 7) >> 1) ^ f) + 16 * (lane & 1); }
    }
    constexpr u32 RPI = PITCH == 256 ? 4 : 8;
    u32 so = sbase, ld = lbase + ldst;
#pragma unroll 1
    for (int j = 0; j < n; j += 4) {
#pragma unroll
      for (int i = 0; i < 4; ++i) { dma1(ld, vo[i], rs, so); so += RPI * stride; ld += 1024; }
      if constexpr (PACE) {
        wait_vm<NINF>();
        if (pend >= 0 && j + 4 >= NINF) { lds_set(L, C_FULL + pend); pend = -1; }
      }
    }
    if (flag >= 0) pend = flag;
  }
  __device__ __forceinline__ void drain() { wait_vm<0>(); if (pend >= 0) lds_set(L, C_FULL + pend); pend = -1; }
};
enum { F_MAC = 0, F_GA, F_GB, F_SB = 0, F_CB };
enum { G_MAC = 0, G_GA, G_R };

template <int PITCH> __device__ __forceinline__ void tr_base(int lane, int c, int& a0, int& a1) {
  const int g = lane >> 4, q = (lane & 15) >> 2, p = lane & 3;
  if constexpr (PITCH == 256) {
    a0 = 256 * (8 * g + q) + 16 * ((2 * c + (p >> 1)) ^ ((q << 2) | ((2 * g) & 3))) + 8 * (p & 1);
    a1 = 256 * (8 * g + 4 + q) + 16 * ((2 * c + (p >> 1)) ^ ((q << 2) | ((2 * g + 1) & 3))) + 8 * (p & 1);
  } else {
    const int f = (q >> 1) | ((g & 1) << 1);
    a0 = 128 * (8 * g + q) + 32 * (c ^ f) + 8 * p;
    a1 = a0 + 128 * 4;
  }
}
template <int PITCH, int KS> __device__ __forceinline__ void load_w(lds_t a0, lds_t a1, v4u (&w)[KS]) {
#pragma unroll
  for (int s = 0; s < KS; ++s) w[s] = tr2(a0 + s * 32 * PITCH, a1 + s * 32 * PITCH);
}
template <int KS> __device__ __forceinline__ void load_x(lds_t xf, v4u (&x)[KS]) {
#pragma unroll
  for (int s = 0; s < KS; ++s) x[s] = *(LAS v4u*)(xf + s * 64);
}
template <int KS> __device__ __forceinline__ f4 mma(const v4u (&w)[KS], const v4u (&x)[KS]) {
  f4 a = {0.f, 0.f, 0.f, 0.f}, b = {0.f, 0.f, 0.f, 0.f};
#pragma unroll
  for (int s = 0; s < KS; s += 2) { a = mfma(w[s], x[s], a); b = mfma(w[s + 1], x[s + 1], b); }
  return a + b;
}

__device__ __forceinline__ float silu_f(float x) { return x * __builtin_amdgcn_rcpf(1.f + __builtin_amdgcn_exp2f(-LOG2E * x)); }
__device__ __forceinline__ float gelu_f(float x) {
  const float u = (2.f * 0.7978845608028654f * LOG2E) * (x + 0.044715f * x * x * x);
  return x * (1.f - __builtin_amdgcn_rcpf(__builtin_amdgcn_exp2f(u) + 1.f));
}
__device__ __forceinline__ void ln_fin(float mean, float var, float& m, float& rden) {
  m = rbf(mean);
  rden = __builtin_amdgcn_rcpf(rbf(__builtin_amdgcn_sqrtf(rbf(rbf(var) + 1e-5f))));
}

__global__ void pre_k(unsigned char* ws) { if (threadIdx.x == 0) __hip_atomic_store((GAS u64*)(ws + (5 << 20)), (u64)__builtin_amdgcn_s_memrealtime(), __ATOMIC_RELAXED, __HIP_MEMORY_SCOPE_AGENT); }
__global__ void __launch_bounds__(DTHR) dummy_k(unsigned char* ws) {
  const u64 t0 = __builtin_amdgcn_s_memrealtime();
  if (threadIdx.x == 0 && blockIdx.x == DBLK) __hip_atomic_store((GAS u64*)(ws + (6 << 20)), t0, __ATOMIC_RELAXED, __HIP_MEMORY_SCOPE_AGENT);
}
__global__ void __launch_bounds__(NTHR, 3) mega(Params p) {
  extern __shared__ __attribute__((aligned(16))) unsigned char smem[];
  lds_t L = (lds_t)smem;
  const int tid = threadIdx.x, lane = tid & 63, wave = __builtin_amdgcn_readfirstlane(tid >> 6);
  const int bid = blockIdx.x, isY = (bid >> 3) & 1, gi = (bid >> 4) * 8 + (bid & 7);
  const int l15 = lane & 15, g4 = lane >> 4;

  if (wave == 0) {
    const int cw = 0; (void)cw;
    Loader ld{L, (u32)(size_t)smem, lane, -1};
    STAMP(16);
    const rsrc_t rwm = mkrs(p.wm, 1024u * 6144u * 2u);
    if (!isY) {
      const int x = gi;
      const u32 kvo = (u32)((x & 7) * 32768 + 28672 + 256 * (x >> 3)) * 256u;
      const rsrc_t rk = mkrs(p.kc, 8u * 32768u * 256u), rv = mkrs(p.vc, 8u * 32768u * 256u);
      ld.seg<256, false>(rk, 256, kvo, 0, 16, -1);
      bar();
      ld.seg<256>(rk, 256, kvo + 64 * 256, 16384, 48, -1);
      STAMP(17);
      ld.seg<256>(rv, 256, kvo, 65536, 64, -1);
      ld.drain();
      STAMP(18);
      bar();
      bar();
      STAMP(11);
      bar();
      bar();
      STAMP(12);
      STAMP(19);
      const u32 mo = (u32)(128 * (x >> 4)) * 12288u + (u32)(x & 15) * 128u;
      ld.seg<128>(rwm, 12288, mo + 3072 * 2, 0, 16, F_SB);
      ld.seg<128>(rwm, 12288, mo + 4096 * 2, 16384, 16, F_CB);
      if (x < 112) {
        const rsrc_t r1 = mkrs(p.w1, 1024u * 3584u * 2u);
        ld.seg<128>(r1, 7168, (u32)(512 * (x & 1)) * 7168u + (u32)(x >> 1) * 128u, 65536, 64, -1);
        ld.drain();
        STAMP(20);
        bar();
        bar();
        bar();
        bar();
      STAMP(13);
      } else ld.drain();
    } else {
      const int y = gi;
      const u32 mco = (u32)(256 * (y >> 5)) * 12288u + (u32)(y & 31) * 128u;
      ld.seg<128, false>(rwm, 12288, mco, 65536, 16, -1);
      bar();
      ld.seg<128>(rwm, 12288, mco + 128 * 12288, 65536 + 16384, 16, F_MAC);
      const rsrc_t rq = mkrs(p.wq, 1024u * 4096u * 2u);
      ld.seg<256>(rq, 8192, (u32)(256 * ((y & 7) >> 1)) * 8192u + (u32)((y >> 3) * 2 + (y & 1)) * 256u, 0, 64, -1);
      ld.drain();
      STAMP(17);
      bar();
      __builtin_amdgcn_s_sleep(2);
      const u32 mo = (u32)(128 * (y >> 4)) * 12288u + (u32)(y & 15) * 128u;
      ld.seg<128>(rwm, 12288, mo + 2048 * 2, L_GT, 16, F_GA);
      const rsrc_t ro = mkrs(p.wo, 4096u * 1024u * 2u);
      const u32 oo = (u32)(128 * ((y & 7) + 8 * (y >> 5))) * 2048u + (u32)((y >> 3) & 3) * 512u;
      lds_wait_ge(L, C_GATE + G_MAC, 4u);
      ld.seg<256>(ro, 2048, oo, 65536, 32, -1);
      ld.seg<256>(ro, 2048, oo + 256, 65536 + 32768, 32, -1);
      ld.drain();
      STAMP(18);
      bar();
      bar();
      STAMP(14);
      if (y < 64) lds_wait_ge(L, C_GATE + G_R, 2u);
      STAMP(19);
      lds_wait_ge(L, C_GATE + G_GA, 4u);
      ld.seg<128>(rwm, 12288, mo + 5120 * 2, L_GT, 16, F_GB);
      if (y < 112) {
        const rsrc_t r2 = mkrs(p.w2, 3584u * 1024u * 2u);
        const u32 o2 = (u32)(128 * (y >> 2)) * 2048u + (u32)(y & 3) * 512u;
        ld.seg<256>(r2, 2048, o2, 0, 32, -1);
        ld.seg<256>(r2, 2048, o2 + 256, 32768, 32, -1);
        ld.drain();
        STAMP(20);
        bar();
        bar();
      } else ld.drain();
    }
    return;
  }

  const int cw = wave - 1, ct = tid - 64;
  const rsrc_t rs = mkrs(p.ws, WS_END);
  if (ct < 64) *(LAS u32*)(L + L_CTL + 4 * ct) = 0u;
  const u64 epoch = __hip_atomic_load((GAS u64*)(p.ws + WS_EPOCH), __ATOMIC_RELAXED, __HIP_MEMORY_SCOPE_AGENT) + 1ull;
  const v2u cin = *(const GAS v2u*)(p.c + (ct >> 8) * 1024 + (ct & 255) * 4);
  const bool pl = l15 < 2;
  const int pub = l15 * 64 + g4 * 16;
  const int xfl = (l15 & 1) * XF_P + g4 * 16;
  lds_t cf_lane = L + L_CF + (l15 & 1) * CF_P + g4 * 16;
  auto silu_to_lds = [&]() {
    *(LAS v2u*)(L + L_CF + (ct >> 8) * CF_P + (ct & 255) * 8) = v2u{pk2(silu_f(bflo(cin.x)), silu_f(bfhi(cin.x))), pk2(silu_f(bflo(cin.y)), silu_f(bfhi(cin.y)))};
  };
  auto raise_flag = [&]() {
    wait_vm<0>();
    u32 old = 0;
    if (lane == 0) old = __hip_atomic_fetch_add((LAS u32*)(L + L_CTL + 4 * C_CLR), 1u, __ATOMIC_RELAXED, __HIP_MEMORY_SCOPE_WORKGROUP);
    if (__builtin_amdgcn_readfirstlane(old) == 7u && lane == 0) __hip_atomic_store((GAS u64*)(p.ws + WS_READY) + bid, epoch, __ATOMIC_RELAXED, __HIP_MEMORY_SCOPE_AGENT);
  };

  if (!isY) {
    const int x = gi, g = x & 7, rec = g * 16 + (x >> 3);
    int o_o3 = WS_O3 + rec * 2048 + ((cw & 1) * 64 + lane) * 16;
    int o_ml = WS_ML + rec * 128 + (lane & 7) * 8;
    int o_sc = WS_PG + (((1 + (cw >> 2)) * 64 + 4 * (x & 15) + (cw & 3)) * 8 + (x >> 4)) * 128 + pub;
    int o_p6 = WS_P6 + ((x >> 1) * 4 + (x & 1) * 2 + (cw >> 2)) * 512 + l15 * 256 + (cw & 3) * 64 + g4 * 16;
    if (cw < 2) st16(rs, o_o3, E4);
    if (cw == 2 && lane < 8) st8(rs, o_ml, E2);
    if (pl) st16(rs, o_sc, E4);
    if (x < 112 && pl) st16(rs, o_p6, E4);
    silu_to_lds();
    bar();
    raise_flag();
    int qoff[2];
    {
      const int base = WS_P2 + (((g >> 2) * 32 + 8 * (g & 3) + cw) * 4) * 512 + (lane & 31) * 16;
      qoff[0] = base + (lane >> 5) * 512; qoff[1] = base + ((lane >> 5) + 2) * 512;
    }
    lds_t kb = L + 256 * (32 * cw + l15) + 16 * (g4 ^ (l15 >> 2));
    lds_t qw = L + L_QF + cw * QF_P + (lane & 31) * 8 + (lane >> 5) * 4;
    lds_t qb = L + L_QF + (l15 & 7) * QF_P + g4 * 16;
    lds_t vb = L + 65536 + 256 * (4 * g4 + (l15 >> 2)) + 16 * ((2 * cw + ((lane & 3) >> 1)) ^ (((l15 >> 2) << 2) | g4)) + 8 * (lane & 1);
    lds_t pfw = L + L_PF + cw * 1024 + lane * 16, pfr = L + L_PF + lane * 16;
    lds_t mww = L + L_MW + l15 * 32 + cw * 4, mwr = L + L_MW + l15 * 32;
    lds_t otw = L + L_OT + (l15 & 7) * QF_P + (16 * cw + 4 * g4) * 2, otr = L + L_OT + (((cw & 1) * 64 + lane) >> 4) * QF_P + l15 * 16;
    PIN(qoff[0]); PIN(qoff[1]); PIN(kb); PIN(qw); PIN(qb); PIN(vb); PIN(pfw); PIN(pfr); PIN(mww); PIN(mwr); PIN(otw); PIN(otr); PIN(o_o3); PIN(o_ml);
    if (cw == 7) { __builtin_amdgcn_s_sleep(64); wait_ready(rs, lane, epoch); lds_set(L, C_RDY); }
    lds_wait_ge(L, C_RDY, 1u);
    bar();
    STAMP(1);
    {
      v4u kf[8], vf[8];
#pragma unroll
      for (int s = 0; s < 4; ++s) { const int ko = 64 * (s ^ (l15 & 3)); kf[s] = *(LAS v4u*)(kb + ko); kf[4 + s] = *(LAS v4u*)(kb + ko + 4096); }
#pragma unroll
      for (int w = 0; w < 8; ++w) vf[w] = tr2(vb + w * 8192, vb + w * 8192 + 4096);
      v4u v[2];
      gather16<2>(rs, qoff, v);
      STAMP(2);
      {
        const f4 a = u2f(v[0]) + u2f(v[1]);
        *(LAS u32*)qw = pk2(sw32(a.x, a.z), sw32(a.y, a.w));
      }
      bar();
      STAMP(11);
      f4 d0 = {0.f, 0.f, 0.f, 0.f}, d1 = {0.f, 0.f, 0.f, 0.f};
#pragma unroll
      for (int s = 0; s < 4; ++s) {
        const v4u qv = *(LAS v4u*)(qb + s * 64);
        d0 = mfma(kf[s], qv, d0);
        d1 = mfma(kf[4 + s], qv, d1);
      }
      float sc[8];
#pragma unroll
      for (int j = 0; j < 2; ++j) {
        const u32 a = pk2(d0[2 * j], d0[2 * j + 1]), b = pk2(d1[2 * j], d1[2 * j + 1]);
        const u32 c = pk2(bflo(a) * 0.08838834764831845f, bfhi(a) * 0.08838834764831845f), d = pk2(bflo(b) * 0.08838834764831845f, bfhi(b) * 0.08838834764831845f);
        sc[2 * j] = bflo(c); sc[2 * j + 1] = bfhi(c); sc[4 + 2 * j] = bflo(d); sc[5 + 2 * j] = bfhi(d);
      }
      float m = fmaxf(fmaxf(fmaxf(sc[0], sc[1]), fmaxf(sc[2], sc[3])), fmaxf(fmaxf(sc[4], sc[5]), fmaxf(sc[6], sc[7])));
      m = mx32(mx16(m));
#pragma unroll
      for (int j = 0; j < 8; ++j) sc[j] = __builtin_amdgcn_exp2f((sc[j] - m) * LOG2E);
      *(LAS v4u*)pfw = v4u{pk2(sc[0], sc[1]), pk2(sc[2], sc[3]), pk2(sc[4], sc[5]), pk2(sc[6], sc[7])};
      {
        float l = ((sc[0] + sc[1]) + (sc[2] + sc[3])) + ((sc[4] + sc[5]) + (sc[6] + sc[7]));
        l = sw16(l, l); l = sw32(l, l);
        if (g4 == 0) { *(LAS float*)mww = m; *(LAS float*)(mww + 512) = l; }
      }
      bar();
      STAMP(3);
      const f4 m0 = *(LAS f4*)mwr, m1 = *(LAS f4*)(mwr + 16);
      f4 d[8];
#pragma unroll
      for (int w = 0; w < 8; ++w) { const f4 z = {0.f, 0.f, 0.f, 0.f}; d[w] = mfma(vf[w], *(LAS v4u*)(pfr + w * 1024), z); }
      m = fmaxf(fmaxf(fmaxf(m0.x, m0.y), fmaxf(m0.z, m0.w)), fmaxf(fmaxf(m1.x, m1.y), fmaxf(m1.z, m1.w)));
      float f[8];
#pragma unroll
      for (int j = 0; j < 4; ++j) { f[j] = __builtin_amdgcn_exp2f((m0[j] - m) * LOG2E); f[4 + j] = __builtin_amdgcn_exp2f((m1[j] - m) * LOG2E); }
      const f4 acc = (((f[0] * d[0] + f[1] * d[1]) + (f[2] * d[2] + f[3] * d[3])) + ((f[4] * d[4] + f[5] * d[5]) + (f[6] * d[6] + f[7] * d[7])));
      if (l15 < 8) *(LAS v2u*)otw = v2u{sane(pk2(acc.x, acc.y)), sane(pk2(acc.z, acc.w))};
      if (cw == 2) {
        const f4 l0 = *(LAS f4*)(mwr + 512), l1 = *(LAS f4*)(mwr + 528);
        const float l = ((f[0] * l0.x + f[1] * l0.y) + (f[2] * l0.z + f[3] * l0.w)) + ((f[4] * l1.x + f[5] * l1.y) + (f[6] * l1.z + f[7] * l1.w));
        if (lane < 8) st8(rs, o_ml, v2u{fsane(m), fsane(l)});
      }
      bar();
      STAMP(12);
      if (cw < 2) st16(rs, o_o3, *(LAS v4u*)otr);
    }
    STAMP(4);
    {
      int a0, a1;
      tr_base<128>(lane, cw & 3, a0, a1);
      v4u xv[4], w[4];
      load_x<4>(cf_lane + (x >> 4) * 256, xv);
      lds_wait_ge(L, C_FULL + F_SB + (cw >> 2), 1u);
      load_w<128, 4>(L + (cw >> 2) * 16384 + a0, L + (cw >> 2) * 16384 + a1, w);
      const f4 a = mma<4>(w, xv);
      if (pl) st16(rs, o_sc, f2u(a));
    }
    STAMP(5);
    if (x < 112) {
      const int b = cw >> 2, k = 256 * (cw & 3) + 4 * lane;
      const int ro = (16 * (cw & 3) + (lane >> 2)) * 128 + b * 32 + (lane & 3) * 8;
      int offs[2] = {WS_SC + ro, WS_SC + ro + 64}, offx[1] = {WS_X1 + ro};
      int a0, a1;
      tr_base<128>(lane, cw & 3, a0, a1);
      lds_t wa0 = L + 65536 + (cw >> 2) * 32768 + a0, wa1 = L + 65536 + (cw >> 2) * 32768 + a1;
      lds_t xr = L + L_XF + xfl + (cw >> 2) * 512, xw = L + L_XF + b * XF_P + (k & 511) * 2;
      lds_t redw = L + L_RED + cw * 8 + (g4 & 1) * 4, redr = L + L_RED + b * 32, x1w = L + L_X1 + b * 2048 + k * 2;
      PIN(offs[0]); PIN(offs[1]); PIN(offx[0]); PIN(wa0); PIN(wa1); PIN(xr); PIN(xw); PIN(redw); PIN(redr); PIN(x1w); PIN(o_p6);
      bar();
      STAMP(6);
      v4u w[8];
      load_w<128, 8>(wa0, wa1, w);
      v2u vs[2], vx[1];
      gather8<2>(rs, offs, vs);
      const float sb[4] = {bflo(vs[0].x), bfhi(vs[0].x), bflo(vs[0].y), bfhi(vs[0].y)};
      const u32 c01 = pk2(1.f + bflo(vs[1].x), 1.f + bfhi(vs[1].x)), c23 = pk2(1.f + bflo(vs[1].y), 1.f + bfhi(vs[1].y));
      const float cb[4] = {bflo(c01), bfhi(c01), bflo(c23), bfhi(c23)};
      gather8<1>(rs, offx, vx);
      STAMP(7);
      float e[4] = {bflo(vx[0].x), bfhi(vx[0].x), bflo(vx[0].y), bfhi(vx[0].y)};
#pragma unroll
      for (int pass = 0; pass < 2; ++pass) {
        float s1 = row_sum16((e[0] + e[1]) + (e[2] + e[3])), s2 = row_sum16((e[0] * e[0] + e[1] * e[1]) + (e[2] * e[2] + e[3] * e[3]));
        float s = sw16(s1, s2);
        s = sw32(s, s);
        *(LAS float*)(redw + pass * 64) = s;
        if (pass == 0) *(LAS v2u*)x1w = vx[0];
        bar();
        const f4 r0 = *(LAS f4*)(redr + pass * 64), r1 = *(LAS f4*)(redr + pass * 64 + 16);
        const float mean = ((r0.x + r0.z) + (r1.x + r1.z)) * (1.f / 1024.f);
        const float var = ((r0.y + r0.w) + (r1.y + r1.w)) * (1.f / 1024.f) - mean * mean;
        float m, rden;
        ln_fin(mean, fmaxf(var, 0.f), m, rden);
        const u32 d01 = pk2(e[0] - m, e[1] - m), d23 = pk2(e[2] - m, e[3] - m);
        u32 z01 = pk2(bflo(d01) * rden, bfhi(d01) * rden), z23 = pk2(bflo(d23) * rden, bfhi(d23) * rden);
        if (pass == 0) {
          const u32 t01 = pk2(bflo(z01) * cb[0], bfhi(z01) * cb[1]), t23 = pk2(bflo(z23) * cb[2], bfhi(z23) * cb[3]);
          z01 = pk2(bflo(t01) + sb[0], bfhi(t01) + sb[1]); z23 = pk2(bflo(t23) + sb[2], bfhi(t23) + sb[3]);
          e[0] = bflo(z01); e[1] = bfhi(z01); e[2] = bflo(z23); e[3] = bfhi(z23);
        } else if (((cw & 3) >> 1) == (x & 1)) *(LAS v2u*)xw = v2u{z01, z23};
      }
      bar();
      STAMP(13);
      v4u xv[8];
      load_x<8>(xr, xv);
      const f4 a = mma<8>(w, xv);
      if (pl) st16(rs, o_p6, f2u(a));
      STAMP(8);
    }
    if (x < 64 && cw == 0) {
      const int ch = lane & 7, hi = lane >> 3;
      const v2u b2v = *(const GAS v2u*)(p.b2 + 16 * x + 4 * (ch & 3));
      int offg[1] = {WS_PG + ((3 * 64 + x) * 8 + hi) * 128 + ch * 16};
      const int pb = WS_P7 + x * 28 * 128 + ch * 16;
      int off[4] = {pb + hi * 128, pb + (hi + 8) * 128, pb + (hi + 16) * 128, pb + (hi < 4 ? hi + 24 : 0) * 128};
      v4u vg[1];
      gather16<1>(rs, offg, vg);
      float gb = rows4(u2f(vg[0]));
      gb = rbf(gb + dpp<ROR8>(gb));
      const int col = 16 * x + 4 * (ch & 3) + g4;
      const float x1 = bflo(*(LAS u16*)(L + L_X1 + (ch >> 2) * 2048 + col * 2));
      const float b2 = g4 == 0 ? bflo(b2v.x) : g4 == 1 ? bfhi(b2v.x) : g4 == 2 ? bflo(b2v.y) : bfhi(b2v.y);
      GAS u16* op = (GAS u16*)p.out + (ch >> 2) * 1024 + col;
      v4u v[4];
      gather16<4>(rs, off, v);
      STAMP(9);
      f4 a = (u2f(v[0]) + u2f(v[1])) + u2f(v[2]);
      if (hi < 4) a += u2f(v[3]);
      float s = rows4(a);
      s += dpp<ROR8>(s);
      const float o = x1 + rbf(gb * rbf(rbf(s) + b2));
      if ((lane & 8) == 0) *op = (u16)pk2(o, o);
      if (x == 0 && lane == 0) __hip_atomic_store((GAS u64*)(p.ws + WS_EPOCH), epoch, __ATOMIC_RELAXED, __HIP_MEMORY_SCOPE_AGENT);
      STAMP(10);
    }
  } else {
    const int y = gi;
    const int qks = (y & 7) >> 1, qh = (y >> 3) * 2 + (y & 1);
    const int oh = (y & 7) + 8 * (y >> 5), ons = (y >> 3) & 3;
    int o_mac = WS_P1 + ((y & 31) * 4 + (y >> 5)) * 512 + l15 * 256 + (cw & 3) * 64 + g4 * 16;
    int o_p2 = WS_P2 + ((l15 * 32 + qh) * 4 + qks) * 512 + cw * 64 + g4 * 16;
    int o_g = WS_PG + ((4 * (y & 15) + (cw & 3)) * 8 + (y >> 4)) * 128 + pub;
    const int rblk = (cw >> 2) * 8 + 2 * (cw & 3);
    int o_p4 = WS_P4 + ((ons * 16 + rblk) * 32 + oh) * 128 + pub;
    int o_p7 = WS_P7 + (((y & 3) * 16 + rblk) * 28 + (y >> 2)) * 128 + pub;
    int o_r = (cw == 0 ? WS_X1 : WS_SC) + y * 128 + lane * 8;
    if (pl) {
      if (cw < 4) st16(rs, o_mac, E4);
      else { st16(rs, o_g, E4); st16(rs, o_g + 3 * 64 * 8 * 128, E4); }
      st16(rs, o_p2, E4);
      st16(rs, o_p4, E4); st16(rs, o_p4 + 32 * 128, E4);
      if (y < 112) { st16(rs, o_p7, E4); st16(rs, o_p7 + 28 * 128, E4); }
    }
    if (y < 64 && ((cw == 0 && lane < 8) || (cw == 1 && lane < 16))) st8(rs, o_r, E2);
    const GAS v4u* xg = (const GAS v4u*)p.x;
    const v4u xa0 = xg[lane * 2], xa1 = xg[lane * 2 + 1], xb0 = xg[128 + lane * 2], xb1 = xg[128 + lane * 2 + 1];
    const int hb = (lane >> 3) & 1, hk = 32 * cw + 4 * (lane & 7) + g4;
    const u32 xq = ((const GAS u16*)p.x)[hb * 1024 + 256 * qks + hk];
    silu_to_lds();
    bar();
    raise_flag();
    if (cw < 4) {
      int a0, a1;
      tr_base<128>(lane, cw, a0, a1);
      v4u xv[8], w[8];
      load_x<8>(cf_lane + (y >> 5) * 512, xv);
      lds_t wa0 = L + 65536 + a0, wa1 = L + 65536 + a1;
      PIN(wa0); PIN(wa1); PIN(o_mac);
      lds_wait_ge(L, C_FULL + F_MAC, 1u);
      STAMP(1);
      load_w<128, 8>(wa0, wa1, w);
      const f4 a = mma<8>(w, xv);
      if (pl) st16(rs, o_mac, f2u(a));
      lds_add(L, C_GATE + G_MAC, lane);
      STAMP(2);
    }
    float ln;
    {
      float m[2], rd[2];
#pragma unroll
      for (int b = 0; b < 2; ++b) {
        const v4u u0 = b ? xb0 : xa0, u1 = b ? xb1 : xa1;
        float e[16];
#pragma unroll
        for (int j = 0; j < 4; ++j) { e[2 * j] = bflo(u0[j]); e[2 * j + 1] = bfhi(u0[j]); e[8 + 2 * j] = bflo(u1[j]); e[9 + 2 * j] = bfhi(u1[j]); }
        float s = 0.f;
#pragma unroll
        for (int j = 0; j < 16; ++j) s += e[j];
        s = row_sum16(s); s = sw16(s, s); s = sw32(s, s);
        const float mean = s * (1.f / 1024.f);
        float s2 = 0.f;
#pragma unroll
        for (int j = 0; j < 16; ++j) { const float d = e[j] - mean; s2 += d * d; }
        s2 = row_sum16(s2); s2 = sw16(s2, s2); s2 = sw32(s2, s2);
        ln_fin(mean, s2 * (1.f / 1024.f), m[b], rd[b]);
      }
      ln = rbf(rbf(bflo(xq) - (hb ? m[1] : m[0])) * (hb ? rd[1] : rd[0]));
    }
    {
      int off[2];
      off[0] = WS_P1 + ((4 * qks + (cw >> 1)) * 4 + g4) * 512 + hb * 256 + (32 * (cw & 1) + 4 * (lane & 7)) * 4;
      off[1] = off[0] + 16 * 4 * 512;
      int a0, a1;
      tr_base<256>(lane, cw, a0, a1);
      lds_t wa0 = L + a0, wa1 = L + a1, xw = L + L_XF + hb * XF_P + hk * 2, xr = L + L_XF + xfl;
      PIN(ln); PIN(off[0]); PIN(off[1]); PIN(wa0); PIN(wa1); PIN(xw); PIN(xr); PIN(o_p2);
      if (cw == 7) { wait_ready(rs, lane, epoch); lds_set(L, C_RDY); }
      lds_wait_ge(L, C_RDY, 1u);
      bar();
      STAMP(3);
      v4u v[2], w[8];
      v[0] = ld16(rs, off[0]); v[1] = ld16(rs, off[1]);
      SCHED();
      load_w<256, 8>(wa0, wa1, w);
      if (!wave_full(umax(mx4(v[0]), mx4(v[1])))) gather16<2>(rs, off, v);
      STAMP(4);
      const u32 sc = pk2(rows4(u2f(v[0])), rows4(u2f(v[1])));
      const float h = rbf(ln * rbf(1.f + bfhi(sc))) + bflo(sc);
      *(LAS u16*)xw = (u16)pk2(h, h);
      flag_sync(L, cw, lane);
      STAMP(15);
      v4u xv[8];
      load_x<8>(xr, xv);
      const f4 a = mma<8>(w, xv);
      if (pl) st16(rs, o_p2, f2u(a));
      STAMP(5);
    }
    int ga0, ga1;
    tr_base<128>(lane, cw & 3, ga0, ga1);
    if (cw >= 4) {
      v4u xv[4], w[4];
      load_x<4>(cf_lane + (y >> 4) * 256, xv);
      lds_wait_ge(L, C_FULL + F_GA, 1u);
      load_w<128, 4>(L + L_GT + ga0, L + L_GT + ga1, w);
      const f4 a = mma<4>(w, xv);
      if (pl) st16(rs, o_g, f2u(a));
      lds_add(L, C_GATE + G_GA, lane);
    }
    int ta0, ta1, tb0, tb1;
    tr_base<256>(lane, 2 * (cw & 3), ta0, ta1);
    tr_base<256>(lane, 2 * (cw & 3) + 1, tb0, tb1);
    {
      const int b = (cw >> 1) & 1, dh = cw & 1, g = b * 4 + (oh >> 3), hh = oh & 7, s0 = lane >> 3;
      int ob = WS_O3 + ((g * 16 + s0) * 8 + hh) * 256 + dh * 128 + (lane & 7) * 16;
      int mb = WS_ML + (g * 16 + s0) * 128 + hh * 8;
      lds_t tile = L + 65536 + (cw >> 2) * 32768;
      lds_t pa0 = tile + ta0, pa1 = tile + ta1, pb0 = tile + tb0, pb1 = tile + tb1;
      lds_t xw = L + L_XF + 2 * XF_P + b * XF_P + (64 * dh + 8 * (lane & 7) + 2 * g4) * 2, xr = L + L_XF + 2 * XF_P + xfl;
      PIN(ob); PIN(mb); PIN(pa0); PIN(pa1); PIN(pb0); PIN(pb1); PIN(xw); PIN(xr); PIN(o_p4);
      bar();
      STAMP(6);
      v4u wa[4], wb[4];
      load_w<256, 4>(pa0, pa1, wa);
      load_w<256, 4>(pb0, pb1, wb);
      if (cw < 4) {
        v4u v[2]; v2u ml[2];
#pragma unroll 1
        for (u32 sp = 0; sp < SPIN_G; ++sp) {
          ml[0] = ld8(rs, mb); ml[1] = ld8(rs, mb + 8 * 128);
          v[0] = ld16(rs, ob); v[1] = ld16(rs, ob + 8 * 2048);
          if (wave_full(umax(umax(mx2(ml[0]), mx2(ml[1])), umax(mx4(v[0]), mx4(v[1]))))) break;
          __builtin_amdgcn_s_sleep(MK_RSLEEP);
        }
        STAMP(7);
        const float m0 = asf(ml[0].x), m1 = asf(ml[1].x);
        float M = fmaxf(m0, m1);
        M = fmaxf(M, dpp<ROR8>(M));
        M = mx32(mx16(M));
        const float e0 = __builtin_amdgcn_exp2f((m0 - M) * LOG2E), e1 = __builtin_amdgcn_exp2f((m1 - M) * LOG2E);
        float Ls = e0 * asf(ml[0].y) + e1 * asf(ml[1].y);
        Ls += dpp<ROR8>(Ls);
        Ls = sw16(Ls, Ls); Ls = sw32(Ls, Ls);
        float o[8];
#pragma unroll
        for (int j = 0; j < 4; ++j) {
          o[2 * j] = e0 * bflo(v[0][j]) + e1 * bflo(v[1][j]);
          o[2 * j + 1] = e0 * bfhi(v[0][j]) + e1 * bfhi(v[1][j]);
        }
        float t0 = sw16(sw32(o[0], o[4]), sw32(o[2], o[6])), t1 = sw16(sw32(o[1], o[5]), sw32(o[3], o[7]));
        t0 += dpp<ROR8>(t0); t1 += dpp<ROR8>(t1);
        const float inv = __builtin_amdgcn_rcpf(Ls);
        if ((lane & 8) == 0) *(LAS u32*)xw = pk2(t0 * inv, t1 * inv);
      }
      bar();
      STAMP(14);
      v4u xv[4];
      load_x<4>(xr, xv);
      const f4 a0 = mma<4>(wa, xv), a1 = mma<4>(wb, xv);
      if (pl) { st16(rs, o_p4, f2u(a0)); st16(rs, o_p4 + 32 * 128, f2u(a1)); }
      STAMP(8);
    }
    if (y < 64 && cw < 2) {
      const int ch = lane & 7, hi = lane >> 3;
      if (cw == 0) {
        const u32 xv = *(const GAS u32*)(p.x + (ch >> 2) * 1024 + 16 * y + 4 * (ch & 3) + 2 * (lane >> 5));
        int offg[1] = {WS_PG + ((0 * 64 + y) * 8 + hi) * 128 + ch * 16};
        const int pb = WS_P4 + (y * 32 + hi) * 128 + ch * 16;
        int off[4] = {pb, pb + 8 * 128, pb + 16 * 128, pb + 24 * 128};
        int ost = WS_X1 + y * 128 + ch * 8 + (lane >> 5) * 4;
        v4u vg[1];
        gather16<1>(rs, offg, vg);
        const f4 gg = u2f(vg[0]);
        float g0 = sw32(gg.x, gg.z), g1 = sw32(gg.y, gg.w);
        g0 = sw16(g0, g0); g1 = sw16(g1, g1);
        g0 = rbf(g0 + dpp<ROR8>(g0)); g1 = rbf(g1 + dpp<ROR8>(g1));
        PIN(g0); PIN(g1); PIN(ost);
        v4u v[4];
        gather16<4>(rs, off, v);
        STAMP(9);
        const f4 a = (u2f(v[0]) + u2f(v[1])) + (u2f(v[2]) + u2f(v[3]));
        float s0 = sw32(a.x, a.z), s1 = sw32(a.y, a.w);
        s0 = sw16(s0, s0); s1 = sw16(s1, s1);
        const u32 at = pk2(s0 + dpp<ROR8>(s0), s1 + dpp<ROR8>(s1));
        const u32 t = pk2(g0 * bflo(at), g1 * bfhi(at));
        const u32 o = sane(pk2(bflo(xv) + bflo(t), bfhi(xv) + bfhi(t)));
        if ((lane & 24) == 0) __builtin_amdgcn_raw_buffer_store_b32(o, rs, ost, 0, SC1);
        STAMP(10);
      } else {
        int off[2] = {WS_PG + ((1 * 64 + y) * 8 + hi) * 128 + ch * 16, WS_PG + ((2 * 64 + y) * 8 + hi) * 128 + ch * 16};
        v4u v[2];
        gather16<2>(rs, off, v);
        const f4 a = u2f(v[0]), c = u2f(v[1]);
        float s0 = sw16(sw32(a.x, a.z), sw32(c.x, c.z)), s1 = sw16(sw32(a.y, a.w), sw32(c.y, c.w));
        s0 += dpp<ROR8>(s0); s1 += dpp<ROR8>(s1);
        const int ost = WS_SC + y * 128 + (g4 & 1) * 64 + ch * 8 + (lane >> 5) * 4;
        if ((lane & 8) == 0) __builtin_amdgcn_raw_buffer_store_b32(sane(pk2(s0, s1)), rs, ost, 0, SC1);
      }
      lds_add(L, C_GATE + G_R, lane);
    }
    if (cw >= 4) {
      v4u xv[4], w[4];
      load_x<4>(cf_lane + (y >> 4) * 256, xv);
      lds_wait_ge(L, C_FULL + F_GB, 1u);
      load_w<128, 4>(L + L_GT + ga0, L + L_GT + ga1, w);
      const f4 a = mma<4>(w, xv);
      if (pl) st16(rs, o_g + 3 * 64 * 8 * 128, f2u(a));
    }
    if (y < 112) {
      const int fs = y >> 2;
      const int id = 16 * (cw & 3) + l15, nsl = id >> 5, b = (id >> 4) & 1, quad = id & 15;
      float b1v = bflo(((const GAS u16*)p.b1)[128 * fs + 64 * nsl + 4 * quad + g4]);
      int off[1] = {WS_P6 + ((2 * fs + nsl) * 4 + g4) * 512 + b * 256 + quad * 16};
      lds_t tile = L + (cw >> 2) * 32768;
      lds_t pa0 = tile + ta0, pa1 = tile + ta1, pb0 = tile + tb0, pb1 = tile + tb1;
      lds_t xw = L + L_XF + 4 * XF_P + b * XF_P + (64 * nsl + 4 * quad + g4) * 2, xr = L + L_XF + 4 * XF_P + xfl;
      PIN(b1v); PIN(off[0]); PIN(pa0); PIN(pa1); PIN(pb0); PIN(pb1); PIN(xw); PIN(xr); PIN(o_p7);
      bar();
      STAMP(11);
      v4u wa[4], wb[4];
      load_w<256, 4>(pa0, pa1, wa);
      load_w<256, 4>(pb0, pb1, wb);
      if (cw < 4) {
        v4u v[1];
        gather16<1>(rs, off, v);
        STAMP(12);
        const float hv = gelu_f(rbf(rbf(rows4(u2f(v[0]))) + b1v));
        *(LAS u16*)xw = (u16)pk2(hv, hv);
      }
      bar();
      v4u xv[4];
      load_x<4>(xr, xv);
      const f4 a0 = mma<4>(wa, xv), a1 = mma<4>(wb, xv);
      if (pl) { st16(rs, o_p7, f2u(a0)); st16(rs, o_p7 + 28 * 128, f2u(a1)); }
      STAMP(13);
    }
  }
  if (gi == 0 && isY == 0 && cw == 0) {
    const u64 tp = __hip_atomic_load((GAS u64*)(p.ws + (5 << 20)), __ATOMIC_RELAXED, __HIP_MEMORY_SCOPE_AGENT);
    const u64 td = __hip_atomic_load((GAS u64*)(p.ws + (6 << 20)), __ATOMIC_RELAXED, __HIP_MEMORY_SCOPE_AGENT);
    u64 d = td - tp; if (d > 5000) d = 5000;
    const u64 until = __builtin_amdgcn_s_memrealtime() + 20ull * d;
    for (u32 sp = 0; sp < (1u << 22); ++sp) if (__builtin_amdgcn_s_memrealtime() >= until) break;
  }
}
}

extern "C" void kernel_launch(void* const* d_in, const int* in_sizes, int n_in, void* d_out, int out_size, void* d_ws, size_t ws_size, hipStream_t stream) {
  using namespace mk;
  Params p;
  p.x = (const u16*)d_in[0]; p.kc = (const u16*)d_in[1]; p.vc = (const u16*)d_in[2]; p.wq = (const u16*)d_in[3]; p.wo = (const u16*)d_in[4];
  p.w1 = (const u16*)d_in[6]; p.b1 = (const u16*)d_in[7]; p.w2 = (const u16*)d_in[8]; p.b2 = (const u16*)d_in[9];
  p.c = (const u16*)d_in[10]; p.wm = (const u16*)d_in[11];
  p.out = (u16*)d_out; p.ws = (unsigned char*)d_ws;
  (void)hipFuncSetAttribute((const void*)mega, hipFuncAttributeMaxDynamicSharedMemorySize, LDS_BYTES);
  (void)hipFuncSetAttribute((const void*)dummy_k, hipFuncAttributeMaxDynamicSharedMemorySize, LDS_BYTES);
  hipLaunchKernelGGL(pre_k, dim3(1), dim3(64), 0, stream, p.ws);
  hipLaunchKernelGGL(dummy_k, dim3(DGRID), dim3(DTHR), DLDS, stream, p.ws);
  hipLaunchKernelGGL(mega, dim3(NWG), dim3(NTHR), LDS_BYTES, stream, p);
}
```

```cpp
#include <hip/hip_runtime.h>
#include <stdint.h>
#pragma clang fp contract(off)

namespace mk {
#define GAS __attribute__((address_space(1)))
#define LAS __attribute__((address_space(3)))
typedef unsigned short u16;
typedef unsigned u32;
typedef unsigned long long u64;
typedef u32 v4u __attribute__((ext_vector_type(4)));
typedef u32 v2u __attribute__((ext_vector_type(2)));
typedef float f4 __attribute__((ext_vector_type(4)));
typedef short s4 __attribute__((ext_vector_type(4)));
typedef __bf16 bf8 __attribute__((ext_vector_type(8)));
typedef __bf16 bf2 __attribute__((ext_vector_type(2)));
typedef LAS unsigned char* lds_t;

constexpr int NWG = 256, NTHR = 576;
constexpr u32 EMPTY = 0xFFFFFFFFu;
constexpr int RSRC_W3 = 0x00020000;
constexpr int SC1 = 16;
constexpr u32 SPIN_G = 1u << 17;
constexpr u32 SPIN_L = 1u << 21;
constexpr float LOG2E = 1.4426950408889634f;

constexpr int WS_EPOCH = 0, WS_READY = 256;
constexpr int WS_P1 = 4096;
constexpr int WS_P2 = WS_P1 + 128 * 512;
constexpr int WS_O3 = WS_P2 + 256 * 512;
constexpr int WS_ML = WS_O3 + 128 * 2048;
constexpr int WS_P4 = WS_ML + 128 * 128;
constexpr int WS_PG = WS_P4 + 64 * 32 * 128;
constexpr int WS_X1 = WS_PG + 4 * 64 * 8 * 128;
constexpr int WS_SC = WS_X1 + 4096;
constexpr int WS_P6 = WS_SC + 8192;
constexpr int WS_P7 = WS_P6 + 28 * 4096;
constexpr int WS_END = WS_P7 + 64 * 28 * 128;

constexpr int L_CTL = 131072;
constexpr int L_CF = L_CTL + 256;
constexpr int CF_P = 2048 + 16;
constexpr int L_XF = L_CF + 2 * CF_P;
constexpr int XF_P = 1024 + 16;
constexpr int L_G = L_XF + 6 * XF_P;
constexpr int QF_P = 256 + 16;
constexpr int L_QF = L_G;
constexpr int L_PF = L_QF + 8 * QF_P;
constexpr int L_MW = L_PF + 8192;
constexpr int L_OT = L_MW + 1024;
constexpr int L_RED = L_OT + 8 * QF_P;
constexpr int L_X1 = L_RED + 512;
constexpr int L_XEND = L_X1 + 4096;
constexpr int L_GT = L_G;
constexpr int L_YEND = L_GT + 16384;
constexpr int LDS_BYTES = 160 * 1024;
static_assert(L_XEND <= LDS_BYTES && L_YEND <= LDS_BYTES, "lds");
static_assert((L_CF % 16) == 0 && (L_XF % 16) == 0 && (L_G % 16) == 0 && (L_PF % 16) == 0 && (L_MW % 16) == 0 && (L_OT % 16) == 0 && (L_RED % 16) == 0 && (L_X1 % 16) == 0, "align");
enum { C_FULL = 0  , C_GATE = 16  , C_CLR = 24, C_CF = 27 };
constexpr int NINF = 32;
constexpr int NI_Q = 48;
constexpr int NI_KV = 36;
constexpr int FKEEP = 16;

struct Params {
  const u16 *x, *kc, *vc, *wq, *wo, *w1, *b1, *w2, *b2, *c, *wm;
  u16* out;
  unsigned char* ws;
};

#define PIN(x) asm volatile("" : "+v"(x))
#define SCHED() __builtin_amdgcn_sched_barrier(0)
__device__ __forceinline__ float asf(u32 b) { return __builtin_bit_cast(float, b); }
__device__ __forceinline__ u32 asu(float f) { return __builtin_bit_cast(u32, f); }
__device__ __forceinline__ float bflo(u32 w) { return asf(w << 16); }
__device__ __forceinline__ float bfhi(u32 w) { return asf(w & 0xffff0000u); }
__device__ __forceinline__ u32 pk2(float lo, float hi) { bf2 v = {(__bf16)lo, (__bf16)hi}; return __builtin_bit_cast(u32, v); }
__device__ __forceinline__ float rbf(float f) { return bflo(pk2(f, f)); }
__device__ __forceinline__ u32 umin(u32 a, u32 b) { return a < b ? a : b; }
__device__ __forceinline__ u32 umax(u32 a, u32 b) { return a > b ? a : b; }
__device__ __forceinline__ u32 sane(u32 w) { return umin(w, 0xFFFEFFFFu); }
template <int CTRL> __device__ __forceinline__ float dpp(float x) { return __builtin_bit_cast(float, __builtin_amdgcn_mov_dpp(__builtin_bit_cast(int, x), CTRL, 0xf, 0xf, true)); }
constexpr int XOR1 = 0xB1, XOR2 = 0x4E, XOR7 = 0x141, ROR8 = 0x128, ROR4 = 0x124;
__device__ __forceinline__ float sw32(float a, float b) { auto s = __builtin_amdgcn_permlane32_swap(asu(a), asu(b), false, false); return asf((u32)s[0]) + asf((u32)s[1]); }
__device__ __forceinline__ float sw16(float a, float b) { auto s = __builtin_amdgcn_permlane16_swap(asu(a), asu(b), false, false); return asf((u32)s[0]) + asf((u32)s[1]); }
__device__ __forceinline__ float max3(float a, float b, float c) { float r; asm("v_max3_f32 %0, %1, %2, %3" : "=v"(r) : "v"(a), "v"(b), "v"(c)); return r; }
__device__ __forceinline__ float mx32(float a) { auto s = __builtin_amdgcn_permlane32_swap(asu(a), asu(a), false, false); return max3(asf((u32)s[0]), asf((u32)s[1]), asf((u32)s[1])); }
__device__ __forceinline__ float mx16(float a) { auto s = __builtin_amdgcn_permlane16_swap(asu(a), asu(a), false, false); return max3(asf((u32)s[0]), asf((u32)s[1]), asf((u32)s[1])); }
__device__ __forceinline__ float mx8(float a) { const float b = dpp<ROR8>(a); return max3(a, b, b); }
__device__ __forceinline__ u32 dppu8(u32 x) { return (u32)__builtin_amdgcn_mov_dpp((int)x, ROR8, 0xf, 0xf, true); }
__device__ __forceinline__ f4 fma4(float a, f4 b, f4 c) { return __builtin_elementwise_fma((f4)(a), b, c); }
__device__ __forceinline__ float rows4(f4 v) { return sw16(sw32(v.x, v.z), sw32(v.y, v.w)); }
__device__ __forceinline__ float row_sum16(float x) { x += dpp<XOR1>(x); x += dpp<XOR2>(x); x += dpp<XOR7>(x); x += dpp<ROR8>(x); return x; }
__device__ __forceinline__ float reduce6(float p0, float p1, float p2, float p3, float p4, float p5, int lane) {
  const bool odd = (lane & 1) != 0, b1 = (lane & 2) != 0;
  const float a = (odd ? p1 : p0) + dpp<XOR1>(odd ? p0 : p1), c = (odd ? p3 : p2) + dpp<XOR1>(odd ? p2 : p3);
  float r = (odd ? p5 : p4) + dpp<XOR1>(odd ? p4 : p5);
  float w = (b1 ? c : a) + dpp<XOR2>(b1 ? a : c);
  r += dpp<XOR2>(r);
  w += dpp<ROR4>(w); r += dpp<ROR4>(r);
  w += dpp<ROR8>(w); r += dpp<ROR8>(r);
  const float t = sw16(w, r);
  return sw32(t, t);
}
template <int N> __device__ __forceinline__ float shl(float x) { return dpp<0x100 + N>(x); }
template <int N> __device__ __forceinline__ f4 shl4(f4 v) { return f4{shl<N>(v.x), shl<N>(v.y), shl<N>(v.z), shl<N>(v.w)}; }
__device__ __forceinline__ float hsum(f4 v) { return (v.x + v.y) + (v.z + v.w); }
__device__ __forceinline__ f4 mfma(v4u a, v4u b, f4 c) { return __builtin_amdgcn_mfma_f32_16x16x32_bf16(__builtin_bit_cast(bf8, a), __builtin_bit_cast(bf8, b), c, 0, 0, 0); }
__device__ __forceinline__ v4u tr2(lds_t a0, lds_t a1) {
  s4 r0 = __builtin_amdgcn_ds_read_tr16_b64_v4i16((LAS s4*)a0), r1 = __builtin_amdgcn_ds_read_tr16_b64_v4i16((LAS s4*)a1);
  return __builtin_bit_cast(v4u, __builtin_shufflevector(r0, r1, 0, 1, 2, 3, 4, 5, 6, 7));
}
template <int N> __device__ __forceinline__ void wait_vm() { asm volatile("s_waitcnt vmcnt(%0)" ::"n"(N) : "memory"); }
__device__ __forceinline__ void bar() { asm volatile("s_waitcnt lgkmcnt(0)\n\ts_barrier" ::: "memory"); }
__device__ __forceinline__ u32 lds_peek(lds_t L, int word) { return __builtin_amdgcn_readfirstlane(*(volatile LAS u32*)(L + L_CTL + 4 * word)); }
__device__ __forceinline__ void lds_wait_ge(lds_t L, int word, u32 want) {
#pragma unroll 1
  for (u32 sp = 0; sp < SPIN_L; ++sp) {
    if (lds_peek(L, word) >= want) break;
  }
  asm volatile("" ::: "memory");
}
__device__ __forceinline__ void lds_set(lds_t L, int word) { *(volatile LAS u32*)(L + L_CTL + 4 * word) = 1u; }
__device__ __forceinline__ void lds_add(lds_t L, int word, int lane) {
  if (lane == 0) (void)__hip_atomic_fetch_add((LAS u32*)(L + L_CTL + 4 * word), 1u, __ATOMIC_RELAXED, __HIP_MEMORY_SCOPE_WORKGROUP);
}

typedef __amdgpu_buffer_rsrc_t rsrc_t;
__device__ __forceinline__ rsrc_t mkrs(const void* p, u32 bytes) { return __builtin_amdgcn_make_buffer_rsrc((void*)p, 0, (int)bytes, RSRC_W3); }
__device__ __forceinline__ v4u ld16(rsrc_t rs, int off) { return __builtin_amdgcn_raw_buffer_load_b128(rs, off, 0, SC1); }
__device__ __forceinline__ v2u ld8(rsrc_t rs, int off) { return __builtin_amdgcn_raw_buffer_load_b64(rs, off, 0, SC1); }
__device__ __forceinline__ void st16(rsrc_t rs, int off, v4u v) { __builtin_amdgcn_raw_buffer_store_b128(v, rs, off, 0, SC1); }
__device__ __forceinline__ u32 mx4(v4u v) { return umax(umax(v.x, v.y), umax(v.z, v.w)); }
__device__ __forceinline__ u32 mx2(v2u v) { return umax(v.x, v.y); }
__device__ __forceinline__ bool wave_full(u32 m) { return __builtin_amdgcn_ballot_w64(m == EMPTY) == 0ull; }
__device__ __forceinline__ u32 tg(float f, u32 t) { return (asu(f) & ~3u) | t; }
__device__ __forceinline__ v4u f2u(f4 v, u32 t) { return v4u{tg(v.x, t), tg(v.y, t), tg(v.z, t), tg(v.w, t)}; }
__device__ __forceinline__ u32 off2(u32 a, u32 b, u32 t) { return __builtin_amdgcn_bitop3_b32(a, b, t, 0x7E); }
__device__ __forceinline__ u32 off4(v4u v, u32 t) { return off2(v.x, v.y, t) | off2(v.z, v.w, t); }
__device__ __forceinline__ bool wave_tagged(u32 d) { return __builtin_amdgcn_ballot_w64((d & 3u) != 0u) == 0ull; }
__device__ __forceinline__ f4 u2f(v4u v) { return __builtin_bit_cast(f4, v); }
constexpr v4u E4 = {EMPTY, EMPTY, EMPTY, EMPTY};

template <int N> __device__ __forceinline__ void gather16(rsrc_t rs, const int (&off)[N], v4u (&v)[N], u32 t) {
#pragma unroll 1
  for (u32 sp = 0; sp < SPIN_G; ++sp) {
#pragma unroll
    for (int i = 0; i < N; ++i) v[i] = ld16(rs, off[i]);
    u32 d = 0;
#pragma unroll
    for (int i = 0; i < N; ++i) { d |= off4(v[i], t); v[i] &= ~3u; }
    if (wave_tagged(d)) break;
    __builtin_amdgcn_s_sleep(1);
  }
}
template <int N> __device__ __forceinline__ void gather8(rsrc_t rs, const int (&off)[N], v2u (&v)[N]) {
#pragma unroll 1
  for (u32 sp = 0; sp < SPIN_G; ++sp) {
#pragma unroll
    for (int i = 0; i < N; ++i) v[i] = ld8(rs, off[i]);
    u32 m = mx2(v[0]);
#pragma unroll
    for (int i = 1; i < N; ++i) m = umax(m, mx2(v[i]));
    if (wave_full(m)) break;
    __builtin_amdgcn_s_sleep(1);
  }
}
__device__ __forceinline__ void wait_ready(rsrc_t rs, int lane, u64 ep) {
#pragma unroll 1
  for (u32 sp = 0; sp < SPIN_G; ++sp) {
    bool ok = true;
#pragma unroll
    for (int i = 0; i < 2; ++i) {
      const v4u v = ld16(rs, WS_READY + i * 1024 + lane * 16);
      ok &= (v.x == (u32)ep) & (v.y == (u32)(ep >> 32)) & (v.z == (u32)ep) & (v.w == (u32)(ep >> 32));
    }
    if (__builtin_amdgcn_ballot_w64(!ok) == 0ull) break;
    __builtin_amdgcn_s_sleep(24);
  }
}

__device__ __forceinline__ void dma1(u32 lds, int voff, rsrc_t rs, u32 soff) {
  asm volatile("s_mov_b32 m0, %0\n\ts_nop 0\n\tbuffer_load_dwordx4 %1, %2, %3 offen nt lds" ::"s"(lds), "v"(voff), "s"(rs), "s"(soff) : "memory");
}
struct Loader {
  lds_t L;
  u32 lbase;
  int lane, pend, since;
  template <int PITCH, bool PACE = true, int NI = NINF> __device__ __forceinline__ void seg(rsrc_t rs, u32 stride, u32 sbase, u32 ldst, int n, int flag) {
    int vo[4];
    if constexpr (PITCH == 256) {
#pragma unroll
      for (int j = 0; j < 4; ++j) vo[j] = (lane >> 4) * stride + 16 * ((lane & 15) ^ (((lane >> 4) << 2) | j));
    } else {
#pragma unroll
      for (int j = 0; j < 4; ++j) { const int f = ((lane >> 4) & 1) | ((j & 1) << 1); vo[j] = (lane >> 3) * stride + 32 * (((lane & 7) >> 1) ^ f) + 16 * (lane & 1); }
    }
    constexpr u32 RPI = PITCH == 256 ? 4 : 8;
    u32 so = sbase, ld = lbase + ldst;
#pragma unroll 1
    for (int j = 0; j < n; j += 4) {
#pragma unroll
      for (int i = 0; i < 4; ++i) { dma1(ld, vo[i], rs, so); so += RPI * stride; ld += 1024; }
      if constexpr (PACE) {
        wait_vm<NI>();
        since += 4;
        constexpr int FK = NI < FKEEP ? NI : FKEEP;
        if (pend >= 0 && since >= FK) { wait_vm<FK>(); lds_set(L, C_FULL + pend); pend = -1; }
      }
    }
    if (flag >= 0) { pend = flag; since = 0; }
  }
  __device__ __forceinline__ void drain() { wait_vm<0>(); if (pend >= 0) lds_set(L, C_FULL + pend); pend = -1; }
};
enum { F_MAC = 0, F_GA, F_GB, F_SB = 0 };
enum { G_MAC = 0, G_GA, G_R };

template <int PITCH> __device__ __forceinline__ void tr_base(int lane, int c, int& a0, int& a1) {
  const int g = lane >> 4, q = (lane & 15) >> 2, p = lane & 3;
  if constexpr (PITCH == 256) {
    a0 = 256 * (8 * g + q) + 16 * ((2 * c + (p >> 1)) ^ ((q << 2) | ((2 * g) & 3))) + 8 * (p & 1);
    a1 = 256 * (8 * g + 4 + q) + 16 * ((2 * c + (p >> 1)) ^ ((q << 2) | ((2 * g + 1) & 3))) + 8 * (p & 1);
  } else {
    const int f = (q >> 1) | ((g & 1) << 1);
    a0 = 128 * (8 * g + q) + 32 * (c ^ f) + 8 * p;
    a1 = a0 + 128 * 4;
  }
}
template <int PITCH, int KS> __device__ __forceinline__ void load_w(lds_t a0, lds_t a1, v4u (&w)[KS]) {
#pragma unroll
  for (int s = 0; s < KS; ++s) w[s] = tr2(a0 + s * 32 * PITCH, a1 + s * 32 * PITCH);
  SCHED();
}
template <int KS> __device__ __forceinline__ void load_x(lds_t xf, v4u (&x)[KS]) {
#pragma unroll
  for (int s = 0; s < KS; ++s) x[s] = *(LAS v4u*)(xf + s * 64);
  SCHED();
}
template <int KS> __device__ __forceinline__ f4 mma(const v4u (&w)[KS], const v4u (&x)[KS]) {
  f4 a = {0.f, 0.f, 0.f, 0.f}, b = {0.f, 0.f, 0.f, 0.f};
#pragma unroll
  for (int s = 0; s < KS; s += 2) { a = mfma(w[s], x[s], a); b = mfma(w[s + 1], x[s + 1], b); }
  return a + b;
}

__device__ __forceinline__ float silu_f(float x) { return x * __builtin_amdgcn_rcpf(1.f + __builtin_amdgcn_exp2f(-LOG2E * x)); }
__device__ __forceinline__ float gelu_f(float x) {
  const float u = (2.f * 0.7978845608028654f * LOG2E) * (x + 0.044715f * x * x * x);
  return x * (1.f - __builtin_amdgcn_rcpf(__builtin_amdgcn_exp2f(u) + 1.f));
}
__device__ __forceinline__ void ln_fin(float mean, float var, float& m, float& rden) {
  m = rbf(mean);
  rden = __builtin_amdgcn_rcpf(rbf(__builtin_amdgcn_sqrtf(rbf(rbf(var) + 1e-5f))));
}

__global__ void __launch_bounds__(NTHR, 3) mega(Params p) {
  extern __shared__ __attribute__((aligned(16))) unsigned char smem[];
  lds_t L = (lds_t)smem;
  const int tid = threadIdx.x, lane = tid & 63, wave = __builtin_amdgcn_readfirstlane(tid >> 6);
  const int bid = blockIdx.x, isY = (bid >> 3) & 1, gi = (bid >> 4) * 8 + (bid & 7);
  const int l15 = lane & 15, g4 = lane >> 4;

  if (tid < 64) *(LAS u32*)(L + L_CTL + 4 * tid) = 0u;
  bar();
  if (wave == 0 && isY) {
    Loader l0{L, (u32)(size_t)smem, lane, -1, 0};
    l0.seg<128, false>(mkrs(p.wm, 1024u * 6144u * 2u), 12288, (u32)(256 * (gi >> 5)) * 12288u + (u32)(gi & 31) * 128u, 65536, 32, F_MAC);
    l0.seg<256, true, NI_Q>(mkrs(p.wq, 1024u * 4096u * 2u), 8192, (u32)(256 * ((gi & 7) >> 1)) * 8192u + (u32)((gi >> 3) * 2 + (gi & 1)) * 256u, 0, 64, -1);
  }
  if (wave == 0) {
    Loader ld{L, (u32)(size_t)smem, lane, -1, 0};
    const rsrc_t rwm = mkrs(p.wm, 1024u * 6144u * 2u);
    if (!isY) {
      const int x = gi;
      const u32 kvo = (u32)((x & 7) * 32768 + 28672 + 256 * (x >> 3)) * 256u;
      const rsrc_t rk = mkrs(p.kc, 8u * 32768u * 256u), rv = mkrs(p.vc, 8u * 32768u * 256u);
      __builtin_amdgcn_s_sleep(46);
      ld.seg<256, true, 16>(rk, 256, kvo, 0, 16, -1);
      ld.seg<256, true, NI_KV>(rk, 256, kvo + 64 * 256, 16384, 48, -1);
      ld.seg<256, true, NI_KV>(rv, 256, kvo, 65536, 64, -1);
      ld.drain();
      const u32 mo = (u32)(128 * (x >> 4)) * 12288u + (u32)(x & 15) * 128u + 3072 * 2;
      int vs[2];
#pragma unroll
      for (int j = 0; j < 2; ++j) vs[j] = (lane >> 3) * 12288 + 32 * (((lane & 7) >> 1) ^ (((lane >> 4) & 1) | (j << 1))) + 16 * (lane & 1);
      PIN(vs[0]); PIN(vs[1]);
      auto sc_issue = [&](int i0, int i1) {
#pragma unroll
        for (int i = i0; i < i1; ++i) dma1(ld.lbase + 1024 * i, vs[i & 1], rwm, mo + (i >> 4) * 2048 + (i & 15) * 8 * 12288);
      };
      bar();
      bar();
      sc_issue(0, 12);
      bar();
      sc_issue(12, 20);
      bar();
      sc_issue(20, 32);
      ld.pend = F_SB; ld.since = 0;
      if (x < 112) {
        const rsrc_t r1 = mkrs(p.w1, 1024u * 3584u * 2u);
        ld.seg<256>(r1, 7168, (u32)(256 * (x & 3)) * 7168u + (u32)(x >> 2) * 256u, 65536, 64, -1);
        ld.drain();
        bar();
        bar();
        bar();
      } else ld.drain();
    } else {
      const int y = gi;
      ld.drain();
      bar();
      bar();
      __builtin_amdgcn_s_sleep(4);
      const u32 mo = (u32)(128 * (y >> 4)) * 12288u + (u32)(y & 15) * 128u;
      ld.seg<128>(rwm, 12288, mo + 2048 * 2, L_GT, 16, F_GA);
      const rsrc_t ro = mkrs(p.wo, 4096u * 1024u * 2u);
      const u32 oo = (u32)(128 * ((y & 7) + 8 * (y >> 5))) * 2048u + (u32)((y >> 3) & 3) * 512u;
      lds_wait_ge(L, C_GATE + G_MAC, 4u);
      ld.seg<256>(ro, 2048, oo, 65536, 32, -1);
      ld.seg<256>(ro, 2048, oo + 256, 65536 + 32768, 32, -1);
      const rsrc_t r2 = mkrs(p.w2, 3584u * 1024u * 2u);
      const u32 o2 = (u32)(128 * (y >> 2)) * 2048u + (u32)(y & 3) * 512u;
      ld.drain();
      bar();
      bar();
      if (y >= 112) lds_wait_ge(L, C_GATE + G_R, 2u);
      lds_wait_ge(L, C_GATE + G_GA, 4u);
      ld.seg<128>(rwm, 12288, mo + 5120 * 2, L_GT, 16, F_GB);
      if (y < 112) {
        ld.seg<256>(r2, 2048, o2, 0, 32, -1);
        ld.seg<256>(r2, 2048, o2 + 256, 32768, 32, -1);
        ld.drain();
        bar();
        bar();
      } else ld.drain();
    }
    return;
  }

  const int cw = wave - 1, ct = tid - 64;
  const rsrc_t rs = mkrs(p.ws, WS_END);
  const u64 epoch0 = __hip_atomic_load((GAS u64*)(p.ws + WS_EPOCH), __ATOMIC_RELAXED, __HIP_MEMORY_SCOPE_AGENT);
  const v2u cin = *(const GAS v2u*)(p.c + (ct >> 8) * 1024 + (ct & 255) * 4);
  const u32 tag = ((u32)epoch0 + 1u) & 3u;
  const bool pl = l15 < 2;
  const int pub = l15 * 64 + g4 * 16;
  const int xfl = (l15 & 1) * XF_P + g4 * 16;
  lds_t cf_lane = L + L_CF + (l15 & 1) * CF_P + g4 * 16;
  auto silu_to_lds = [&]() {
    *(LAS v2u*)(L + L_CF + (ct >> 8) * CF_P + (ct & 255) * 8) = v2u{pk2(silu_f(bflo(cin.x)), silu_f(bfhi(cin.x))), pk2(silu_f(bflo(cin.y)), silu_f(bfhi(cin.y)))};
  };
  auto raise_flag = [&](u32 last) {
    wait_vm<0>();
    u32 old = 0;
    if (lane == 0) old = __hip_atomic_fetch_add((LAS u32*)(L + L_CTL + 4 * C_CLR), 1u, __ATOMIC_RELAXED, __HIP_MEMORY_SCOPE_WORKGROUP);
    if (__builtin_amdgcn_readfirstlane(old) == last && lane == 0) __hip_atomic_store((GAS u64*)(p.ws + WS_READY) + bid, epoch0 + 1ull, __ATOMIC_RELAXED, __HIP_MEMORY_SCOPE_AGENT);
  };

  auto raise_flagY = [&]() {
    if (cw == 7) {
      if (gi >= 112) lds_wait_ge(L, C_CLR, 2u);
      if (lane == 0) __hip_atomic_store((GAS u64*)(p.ws + WS_READY) + bid, epoch0 + 1ull, __ATOMIC_RELAXED, __HIP_MEMORY_SCOPE_AGENT);
    }
  };
  auto count_clear = [&]() { wait_vm<0>(); lds_add(L, C_CLR, lane); };
  auto clear_r = [&](int sidx) {
    const int r16 = sidx >> 1, b = sidx & 1, col = 64 * r16 + 4 * l15 + 2 * (lane >> 5);
    if (cw == 0 && (lane & 16) == 0) __builtin_amdgcn_raw_buffer_store_b32(EMPTY, rs, WS_X1 + b * 2048 + col * 2, 0, SC1);
    if (cw == 1) __builtin_amdgcn_raw_buffer_store_b32(EMPTY, rs, WS_SC + ((g4 & 1) * 2 + b) * 2048 + col * 2, 0, SC1);
  };
  auto stage_r = [&](int sidx) {
    if (cw < 2) {
      const int r16 = sidx >> 1, b = sidx & 1, col = 64 * r16 + 4 * l15 + 2 * (lane >> 5);
      const int pg = WS_PG + ((4 * r16 + (l15 >> 2)) * 8 + g4) * 128 + b * 64 + (l15 & 3) * 16;
      if (cw == 0) {
        const u32 xv = *(const GAS u32*)(p.x + b * 1024 + col);
        int offg[2] = {pg, pg + 4 * 128};
        const int pb = WS_P4 + ((r16 * 2 + b) * 32 + g4) * 256 + l15 * 16;
        int off[8];
#pragma unroll
        for (int i = 0; i < 8; ++i) off[i] = pb + i * 1024;
        int ost = WS_X1 + b * 2048 + col * 2;
        v4u vg[2];
        gather16<2>(rs, offg, vg, tag);
        const f4 gg = u2f(vg[0]) + u2f(vg[1]);
        float g0 = sw32(gg.x, gg.z), g1 = sw32(gg.y, gg.w);
        g0 = rbf(sw16(g0, g0)); g1 = rbf(sw16(g1, g1));
        float x0 = bflo(xv), x1 = bfhi(xv);
        PIN(g0); PIN(g1); PIN(ost); PIN(x0); PIN(x1);
        v4u v[8];
        gather16<8>(rs, off, v, tag);
        const f4 a = ((u2f(v[0]) + u2f(v[1])) + (u2f(v[2]) + u2f(v[3]))) + ((u2f(v[4]) + u2f(v[5])) + (u2f(v[6]) + u2f(v[7])));
        const float s0 = sw32(a.x, a.z), s1 = sw32(a.y, a.w);
        const u32 at = pk2(sw16(s0, s0), sw16(s1, s1));
        const u32 t = pk2(g0 * bflo(at), g1 * bfhi(at));
        const u32 o = sane(pk2(x0 + bflo(t), x1 + bfhi(t)));
        if ((lane & 16) == 0) __builtin_amdgcn_raw_buffer_store_b32(o, rs, ost, 0, SC1);
      } else {
        int off[4] = {pg + 1 * 64 * 8 * 128, pg + 1 * 64 * 8 * 128 + 4 * 128, pg + 2 * 64 * 8 * 128, pg + 2 * 64 * 8 * 128 + 4 * 128};
        v4u v[4];
        gather16<4>(rs, off, v, tag);
        const f4 a = u2f(v[0]) + u2f(v[1]), c = u2f(v[2]) + u2f(v[3]);
        const float s0 = sw16(sw32(a.x, a.z), sw32(c.x, c.z)), s1 = sw16(sw32(a.y, a.w), sw32(c.y, c.w));
        __builtin_amdgcn_raw_buffer_store_b32(sane(pk2(s0, s1)), rs, WS_SC + ((g4 & 1) * 2 + b) * 2048 + col * 2, 0, SC1);
      }
      lds_add(L, C_GATE + G_R, lane);
    }
  };

  if (!isY) {
    const int x = gi, g = x & 7, rec = g * 16 + (x >> 3);
    int o_o3 = WS_O3 + rec * 2048 + ((cw & 1) * 64 + lane) * 16;
    int o_ml = WS_ML + rec * 128 + (lane & 7) * 16;
    int o_sc = WS_PG + (((1 + (cw >> 2)) * 64 + 4 * (x & 15) + (cw & 3)) * 8 + (x >> 4)) * 128 + pub;
    int o_p6 = WS_P6 + x * 1024 + cw * 128 + pub;
    if (cw < 2) st16(rs, o_o3, E4);
    if (x >= 112) clear_r(x - 112);
    silu_to_lds();
    lds_add(L, C_CF, lane);
    if (cw < 2) raise_flag(1u);
    int qoff[2];
    {
      const int base = WS_P2 + (((g >> 2) * 32 + 8 * (g & 3) + cw) * 4) * 512 + (lane & 31) * 16;
      qoff[0] = base + (lane >> 5) * 512; qoff[1] = base + ((lane >> 5) + 2) * 512;
    }
    lds_t kb = L + 256 * (32 * cw + l15) + 16 * (g4 ^ (l15 >> 2));
    lds_t qw = L + L_QF + cw * QF_P + (lane & 31) * 8 + (lane >> 5) * 4;
    lds_t qb = L + L_QF + (l15 & 7) * QF_P + g4 * 16;
    lds_t vb = L + 65536 + 256 * (4 * g4 + (l15 >> 2)) + 16 * ((2 * cw + ((lane & 3) >> 1)) ^ (((l15 >> 2) << 2) | g4)) + 8 * (lane & 1);
    lds_t pfw = L + L_PF + cw * 1024 + lane * 16, pfr = L + L_PF + lane * 16;
    lds_t mww = L + L_MW + l15 * 32 + cw * 4, mwr = L + L_MW + l15 * 32;
    lds_t otw = L + L_OT + (l15 & 7) * QF_P + (16 * cw + 4 * g4) * 2, otr = L + L_OT + (((cw & 1) * 64 + lane) >> 4) * QF_P + l15 * 16;
    PIN(qoff[0]); PIN(qoff[1]); PIN(kb); PIN(qw); PIN(qb); PIN(vb); PIN(pfw); PIN(pfr); PIN(mww); PIN(mwr); PIN(otw); PIN(otr); PIN(o_o3); PIN(o_ml);
    bar();
    {
      v4u kf[8], vf[8], v[2];
      v[0] = ld16(rs, qoff[0]); v[1] = ld16(rs, qoff[1]);
      SCHED();
#pragma unroll
      for (int s = 0; s < 4; ++s) { const int ko = 64 * (s ^ (l15 & 3)); kf[s] = *(LAS v4u*)(kb + ko); kf[4 + s] = *(LAS v4u*)(kb + ko + 4096); }
#pragma unroll
      for (int w = 0; w < 8; ++w) vf[w] = tr2(vb + w * 8192, vb + w * 8192 + 4096);
      SCHED();
      { const u32 d = off4(v[0], tag) | off4(v[1], tag); v[0] &= ~3u; v[1] &= ~3u; if (!wave_tagged(d)) gather16<2>(rs, qoff, v, tag); }
      {
        const f4 a = u2f(v[0]) + u2f(v[1]);
        *(LAS u32*)qw = pk2(sw32(a.x, a.z), sw32(a.y, a.w));
      }
      bar();
      v4u qv[4];
      load_x<4>(qb, qv);
      f4 d0 = {0.f, 0.f, 0.f, 0.f}, d1 = {0.f, 0.f, 0.f, 0.f};
#pragma unroll
      for (int s = 0; s < 4; ++s) { d0 = mfma(kf[s], qv[s], d0); d1 = mfma(kf[4 + s], qv[s], d1); }
      float sc[4];
      {
        const bool lo = l15 < 8;
        const u32 a = pk2(lo ? d0.x : d1.x, lo ? d0.y : d1.y), b = pk2(lo ? d0.z : d1.z, lo ? d0.w : d1.w);
        const u32 c = pk2(bflo(a) * 0.08838834764831845f, bfhi(a) * 0.08838834764831845f), d = pk2(bflo(b) * 0.08838834764831845f, bfhi(b) * 0.08838834764831845f);
        sc[0] = bflo(c); sc[1] = bfhi(c); sc[2] = bflo(d); sc[3] = bfhi(d);
      }
      float m = max3(max3(sc[0], sc[1], sc[2]), sc[3], sc[3]);
      m = mx32(mx16(mx8(m)));
      {
        const float nm = -LOG2E * m;
#pragma unroll
        for (int j = 0; j < 4; ++j) sc[j] = __builtin_amdgcn_exp2f(__builtin_fmaf(sc[j], LOG2E, nm));
      }
      {
        const u32 p01 = pk2(sc[0], sc[1]), p23 = pk2(sc[2], sc[3]);
        *(LAS v4u*)pfw = v4u{p01, p23, dppu8(p01), dppu8(p23)};
        if (g4 == 0) *(LAS float*)mww = m;
      }
      bar();
      const f4 m0 = *(LAS f4*)mwr, m1 = *(LAS f4*)(mwr + 16);
      v4u pf[8];
#pragma unroll
      for (int w = 0; w < 8; ++w) pf[w] = *(LAS v4u*)(pfr + w * 1024);
      SCHED();
      {
        float l = (sc[0] + sc[1]) + (sc[2] + sc[3]);
        l += dpp<ROR8>(l);
        l = sw16(l, l); l = sw32(l, l);
        if (g4 == 0) *(LAS float*)(mww + 512) = l;
      }
      f4 d[8];
#pragma unroll
      for (int w = 0; w < 8; ++w) { const f4 z = {0.f, 0.f, 0.f, 0.f}; d[w] = mfma(vf[w], pf[w], z); }
      m = max3(max3(max3(m0.x, m0.y, m0.z), m0.w, m1.x), max3(m1.y, m1.z, m1.w), m1.w);
      float f[8];
      {
        const float nm = -LOG2E * m;
#pragma unroll
        for (int j = 0; j < 4; ++j) { f[j] = __builtin_amdgcn_exp2f(__builtin_fmaf(m0[j], LOG2E, nm)); f[4 + j] = __builtin_amdgcn_exp2f(__builtin_fmaf(m1[j], LOG2E, nm)); }
      }
      const f4 acc = fma4(f[3], d[3], fma4(f[2], d[2], fma4(f[1], d[1], f[0] * d[0]))) + fma4(f[7], d[7], fma4(f[6], d[6], fma4(f[5], d[5], f[4] * d[4])));
      if (l15 < 8) *(LAS v2u*)otw = v2u{sane(pk2(acc.x, acc.y)), sane(pk2(acc.z, acc.w))};
      bar();
      if (cw < 2) st16(rs, o_o3, *(LAS v4u*)otr);
      if (cw == 2) {
        const f4 l0 = *(LAS f4*)(mwr + 512), l1 = *(LAS f4*)(mwr + 528);
        const float l = ((f[0] * l0.x + f[1] * l0.y) + (f[2] * l0.z + f[3] * l0.w)) + ((f[4] * l1.x + f[5] * l1.y) + (f[6] * l1.z + f[7] * l1.w));
        if (lane < 8) st16(rs, o_ml, v4u{asu(m), (u32)(epoch0 + 1ull), asu(l), (u32)((epoch0 + 1ull) >> 32) ^ 0x9E3779B9u});
      }
    }
    {
      int a0, a1;
      tr_base<128>(lane, cw & 3, a0, a1);
      v4u xv[4], w[4];
      load_x<4>(cf_lane + (x >> 4) * 256, xv);
      if (x < 112 || cw != 0) {
        lds_wait_ge(L, C_FULL + F_SB, 1u);
        load_w<128, 4>(L + (cw >> 2) * 16384 + a0, L + (cw >> 2) * 16384 + a1, w);
        const f4 a = mma<4>(w, xv);
        if (pl) st16(rs, o_sc, f2u(a, tag));
        if (x >= 112 && cw == 4) {
          load_w<128, 4>(L + a0, L + a1, w);
          const f4 a2 = mma<4>(w, xv);
          if (pl) st16(rs, o_sc - 64 * 8 * 128, f2u(a2, tag));
        }
      }
    }
    if (x >= 112) stage_r(x - 112);
    if (x < 112) {
      const int b = cw >> 2, k = 256 * (cw & 3) + 4 * lane;
      const bool mine = (cw & 3) == (x & 3);
      constexpr int PP = 528;
      int offs[2] = {WS_SC + b * 2048 + k * 2, WS_SC + (2 + b) * 2048 + k * 2}, offx[1] = {WS_X1 + b * 2048 + k * 2};
      int a0, a1;
      tr_base<256>(lane, cw, a0, a1);
      lds_t wa0 = L + 65536 + a0, wa1 = L + 65536 + a1;
      lds_t xr = L + L_XF + xfl, xw = L + L_XF + b * XF_P + lane * 8;
      lds_t redw = L + L_RED + ((b * 6 + (lane < 16 ? lane : lane - 12)) * 4 + (cw & 3)) * 4;
      lds_t redr = L + L_RED + ((l15 & 1) * 6 + (l15 < 12 ? l15 >> 1 : 0)) * 16, x1w = L + L_X1 + b * 2048 + k * 2;
      const bool redl = lane < 4 || (lane & ~1) == 16;
      PIN(offs[0]); PIN(offs[1]); PIN(offx[0]); PIN(wa0); PIN(wa1); PIN(xr); PIN(xw); PIN(redw); PIN(redr); PIN(x1w); PIN(o_p6);
      if (cw == 3) wait_ready(rs, lane, epoch0 + 1ull);
      bar();
      v4u w[8];
      load_w<256, 8>(wa0, wa1, w);
      v2u vs[2], vx[1];
      gather8<2>(rs, offs, vs);
      const f4 sb = {bflo(vs[0].x), bfhi(vs[0].x), bflo(vs[0].y), bfhi(vs[0].y)};
      const u32 g01 = pk2(1.f + bflo(vs[1].x), 1.f + bfhi(vs[1].x)), g23 = pk2(1.f + bflo(vs[1].y), 1.f + bfhi(vs[1].y));
      f4 g = {bflo(g01), bfhi(g01), bflo(g23), bfhi(g23)};
      if (mine) {
        *(LAS v2u*)(L + L_PF + b * PP + lane * 8) = v2u{g01, g23};
        *(LAS v2u*)(L + L_PF + (2 + b) * PP + lane * 8) = vs[0];
        *(LAS v2u*)(L + L_PF + (4 + b) * PP + lane * 8) = v2u{0x3F803F80u, 0x3F803F80u};
      }
      f4 gg = g * g, gs = g * sb;
      {
        const float t = reduce6(hsum(g), hsum(gg), hsum(sb), hsum(sb * sb), hsum(gs), 0.f, lane);
        if (redl) *(LAS float*)(redw + 256) = t;
      }
      bar();
      f4 Bv, Cv, Dv;
      float G1, G2, S1s, S2s, GS;
      {
        v4u pv[8];
        load_x<8>(L + L_PF + (l15 < 6 ? l15 : 0) * PP + g4 * 16, pv);
        const float tot = hsum(*(LAS f4*)(redr + 256));
        Bv = mma<8>(w, pv);
        Cv = shl4<2>(Bv); Dv = shl4<4>(Bv);
        G1 = tot; G2 = shl<2>(tot); S1s = shl<4>(tot); S2s = shl<6>(tot); GS = shl<8>(tot);
      }
      PIN(Bv); PIN(Cv); PIN(Dv); PIN(G1); PIN(G2); PIN(S1s); PIN(S2s); PIN(GS); PIN(g); PIN(gg); PIN(gs);
      gather8<1>(rs, offx, vx);
      {
        const f4 e = {bflo(vx[0].x), bfhi(vx[0].x), bflo(vx[0].y), bfhi(vx[0].y)};
        const f4 u = g * e;
        if (mine) *(LAS v2u*)xw = v2u{pk2(u.x, u.y), pk2(u.z, u.w)};
        const float t = reduce6(hsum(e), hsum(e * e), hsum(u), hsum(u * u), hsum(gs * e), hsum(gg * e), lane);
        if (redl) *(LAS float*)redw = t;
        *(LAS v2u*)x1w = vx[0];
      }
      bar();
      v4u xv[8];
      const f4 st = *(LAS f4*)redr;
      load_x<8>(xr, xv);
      const float S1 = hsum(st), S2 = shl<2>(S1), T1 = shl<4>(S1), T2 = shl<6>(S1), T3 = shl<8>(S1), T4 = shl<10>(S1);
      constexpr float RN = 1.f / 1024.f;
      const float mu1 = S1 * RN;
      const float r1 = __builtin_amdgcn_rsqf(max3(__builtin_fmaf(-mu1, mu1, S2 * RN), 0.f, 0.f) + 1e-5f);
      const float sh = __builtin_fmaf(r1, __builtin_fmaf(-mu1, G1, T1), S1s);
      const float q1 = __builtin_fmaf(mu1, __builtin_fmaf(mu1, G2, -2.f * T4), T2), q2 = __builtin_fmaf(-mu1, GS, T3);
      const float sh2 = __builtin_fmaf(r1, __builtin_fmaf(r1, q1, 2.f * q2), S2s);
      const float mu2 = sh * RN;
      const float r2 = __builtin_amdgcn_rsqf(max3(__builtin_fmaf(-mu2, mu2, sh2 * RN), 0.f, 0.f) + 1e-5f);
      const f4 E = r2 * fma4(-mu2, Dv, fma4(-r1 * mu1, Bv, Cv));
      const f4 A = mma<8>(w, xv);
      if (pl) st16(rs, o_p6, f2u(fma4(r2 * r1, A, E), tag));
    }
    if (x < 64 && cw == 0) {
      const int ch = lane & 7, hi = lane >> 3;
      const v2u b2v = *(const GAS v2u*)(p.b2 + 16 * x + 4 * (ch & 3));
      int offg[1] = {WS_PG + ((3 * 64 + x) * 8 + hi) * 128 + ch * 16};
      const int pb = WS_P7 + x * 28 * 128 + ch * 16;
      int off[4] = {pb + hi * 128, pb + (hi + 8) * 128, pb + (hi + 16) * 128, pb + (hi < 4 ? hi + 24 : 0) * 128};
      v4u vg[1];
      gather16<1>(rs, offg, vg, tag);
      float gb = rows4(u2f(vg[0]));
      gb = rbf(gb + dpp<ROR8>(gb));
      const int col = 16 * x + 4 * (ch & 3) + g4;
      const float x1 = bflo(*(LAS u16*)(L + L_X1 + (ch >> 2) * 2048 + col * 2));
      const float b2 = g4 == 0 ? bflo(b2v.x) : g4 == 1 ? bfhi(b2v.x) : g4 == 2 ? bflo(b2v.y) : bfhi(b2v.y);
      GAS u16* op = (GAS u16*)p.out + (ch >> 2) * 1024 + col;
      v4u v[4];
      gather16<4>(rs, off, v, tag);
      f4 a = (u2f(v[0]) + u2f(v[1])) + u2f(v[2]);
      if (hi < 4) a += u2f(v[3]);
      float s = rows4(a);
      s += dpp<ROR8>(s);
      const float o = x1 + rbf(gb * rbf(rbf(s) + b2));
      if ((lane & 8) == 0) *op = (u16)pk2(o, o);
      if (x == 0 && lane == 0) __hip_atomic_store((GAS u64*)(p.ws + WS_EPOCH), epoch0 + 1ull, __ATOMIC_RELAXED, __HIP_MEMORY_SCOPE_AGENT);
    }
  } else {
    const int y = gi;
    const int qks = (y & 7) >> 1, qh = (y >> 3) * 2 + (y & 1);
    const int oh = (y & 7) + 8 * (y >> 5), ons = (y >> 3) & 3;
    int o_mac = WS_P1 + ((y & 31) * 4 + (y >> 5)) * 512 + (cw & 3) * 128 + pub;
    int o_p2 = WS_P2 + (((l15 & 1) * 32 + qh) * 4 + qks) * 512 + (cw & 3) * 128 + ((l15 >> 1) & 1) * 64 + g4 * 16;
    int o_g = WS_PG + ((4 * (y & 15) + (cw & 3)) * 8 + (y >> 4)) * 128 + pub;
    const int rblk = (cw >> 2) * 8 + 2 * (cw & 3);
    int o_p4 = WS_P4 + (((4 * ons + 2 * (cw >> 2) + ((cw & 3) >> 1)) * 2 + (l15 & 1)) * 32 + oh) * 256 + (cw & 1) * 128 + ((l15 >> 1) & 1) * 64 + g4 * 16;
    int o_p7 = WS_P7 + (((y & 3) * 16 + rblk) * 28 + (y >> 2)) * 128 + pub;
    if (y >= 112) clear_r(16 + y - 112);
    const GAS v4u* xg = (const GAS v4u*)p.x;
    const v4u xa0 = xg[lane * 2], xa1 = xg[lane * 2 + 1], xb0 = xg[128 + lane * 2], xb1 = xg[128 + lane * 2 + 1];
    const int hb = (lane >> 3) & 1, hk = 32 * cw + 4 * (lane & 7) + g4;
    const u32 xq = ((const GAS u16*)p.x)[hb * 1024 + 256 * qks + hk];
    silu_to_lds();
    lds_add(L, C_CF, lane);
    if (cw == 6 || cw == 7) raise_flagY();
    if (cw < 4) {
      int a0, a1;
      tr_base<128>(lane, cw, a0, a1);
      v4u xv[8], w[8];
      lds_wait_ge(L, C_CF, 8u);
      load_x<8>(cf_lane + (y >> 5) * 512, xv);
      lds_t wa0 = L + 65536 + a0, wa1 = L + 65536 + a1;
      PIN(wa0); PIN(wa1); PIN(o_mac);
      lds_wait_ge(L, C_FULL + F_MAC, 1u);
      load_w<128, 8>(wa0, wa1, w);
      const f4 a = mma<8>(w, xv);
      if (pl) st16(rs, o_mac, f2u(a, tag));
      lds_add(L, C_GATE + G_MAC, lane);
      if (y >= 112 && cw < 2) count_clear();
    }
    float ln;
    {
      float m[2], rd[2];
#pragma unroll
      for (int b = 0; b < 2; ++b) {
        const v4u u0 = b ? xb0 : xa0, u1 = b ? xb1 : xa1;
        float e[16];
#pragma unroll
        for (int j = 0; j < 4; ++j) { e[2 * j] = bflo(u0[j]); e[2 * j + 1] = bfhi(u0[j]); e[8 + 2 * j] = bflo(u1[j]); e[9 + 2 * j] = bfhi(u1[j]); }
        float s = 0.f;
#pragma unroll
        for (int j = 0; j < 16; ++j) s += e[j];
        s = row_sum16(s); s = sw16(s, s); s = sw32(s, s);
        const float mean = s * (1.f / 1024.f);
        float s2 = 0.f;
#pragma unroll
        for (int j = 0; j < 16; ++j) { const float d = e[j] - mean; s2 += d * d; }
        s2 = row_sum16(s2); s2 = sw16(s2, s2); s2 = sw32(s2, s2);
        ln_fin(mean, s2 * (1.f / 1024.f), m[b], rd[b]);
      }
      ln = rbf(rbf(bflo(xq) - (hb ? m[1] : m[0])) * (hb ? rd[1] : rd[0]));
    }
    {
      int off[2];
      off[0] = WS_P1 + ((4 * qks + (cw >> 1)) * 4 + g4) * 512 + (2 * (cw & 1) + ((lane & 7) >> 2)) * 128 + hb * 64 + (lane & 3) * 16;
      off[1] = off[0] + 16 * 4 * 512;
      int a0, a1, b0, b1;
      tr_base<256>(lane, 2 * (cw & 3), a0, a1);
      tr_base<256>(lane, 2 * (cw & 3) + 1, b0, b1);
      lds_t wa0 = L + a0, wa1 = L + a1, wb0 = L + b0, wb1 = L + b1, xw = L + L_XF + hb * XF_P + hk * 2, xr = L + L_XF + xfl;
      PIN(ln); PIN(off[0]); PIN(off[1]); PIN(wa0); PIN(wa1); PIN(wb0); PIN(wb1); PIN(xw); PIN(xr); PIN(o_p2);
      bar();
      v4u v[2], wa[8], wb[8];
      v[0] = ld16(rs, off[0]); v[1] = ld16(rs, off[1]);
      SCHED();
      if (cw < 4) { load_w<256, 8>(wa0, wa1, wa); load_w<256, 8>(wb0, wb1, wb); }
      { const u32 d = off4(v[0], tag) | off4(v[1], tag); v[0] &= ~3u; v[1] &= ~3u; if (!wave_tagged(d)) gather16<2>(rs, off, v, tag); }
      const u32 sc = pk2(rows4(u2f(v[0])), rows4(u2f(v[1])));
      const float h = rbf(ln * rbf(1.f + bfhi(sc))) + bflo(sc);
      *(LAS u16*)xw = (u16)pk2(h, h);
      bar();
      if (cw < 4) {
        v4u xv[8];
        load_x<8>(xr, xv);
        const f4 ra = mma<8>(wa, xv), rb = mma<8>(wb, xv);
        const bool up = (l15 & 2) != 0;
        const f4 a = {up ? rb.x : ra.x, up ? rb.y : ra.y, up ? rb.z : ra.z, up ? rb.w : ra.w};
        if (l15 < 4) st16(rs, o_p2, f2u(a, tag));
      }
    }
    int ga0, ga1;
    tr_base<128>(lane, cw & 3, ga0, ga1);
    if (cw >= 4) {
      v4u xv[4], w[4];
      load_x<4>(cf_lane + (y >> 4) * 256, xv);
      lds_wait_ge(L, C_FULL + F_GA, 1u);
      load_w<128, 4>(L + L_GT + ga0, L + L_GT + ga1, w);
      const f4 a = mma<4>(w, xv);
      if (pl) st16(rs, o_g, f2u(a, tag));
      lds_add(L, C_GATE + G_GA, lane);
    }
    int ta0, ta1, tb0, tb1;
    tr_base<256>(lane, 2 * (cw & 3), ta0, ta1);
    tr_base<256>(lane, 2 * (cw & 3) + 1, tb0, tb1);
    {
      const int b = (cw >> 1) & 1, dh = cw & 1, g = b * 4 + (oh >> 3), hh = oh & 7, s0 = lane >> 3;
      int ob = WS_O3 + ((g * 16 + s0) * 8 + hh) * 256 + dh * 128 + (lane & 7) * 16;
      int mb = WS_ML + (g * 16 + s0) * 128 + hh * 16;
      lds_t tile = L + 65536 + (cw >> 2) * 32768;
      lds_t pa0 = tile + ta0, pa1 = tile + ta1, pb0 = tile + tb0, pb1 = tile + tb1;
      lds_t xw = L + L_XF + 2 * XF_P + b * XF_P + (64 * dh + 8 * (lane & 7) + 2 * g4) * 2, xr = L + L_XF + 2 * XF_P + xfl;
      PIN(ob); PIN(mb); PIN(pa0); PIN(pa1); PIN(pb0); PIN(pb1); PIN(xw); PIN(xr); PIN(o_p4);
      if (cw == 3) wait_ready(rs, lane, epoch0 + 1ull);
      bar();
      v4u wa[4], wb[4];
      load_w<256, 4>(pa0, pa1, wa);
      load_w<256, 4>(pb0, pb1, wb);
      if (cw < 4) {
        v4u v[2], ml[2];
        const u32 ta = (u32)(epoch0 + 1ull), tb = (u32)((epoch0 + 1ull) >> 32) ^ 0x9E3779B9u;
#pragma unroll 1
        for (u32 sp = 0; sp < SPIN_G; ++sp) {
          ml[0] = ld16(rs, mb); ml[1] = ld16(rs, mb + 8 * 128);
          v[0] = ld16(rs, ob); v[1] = ld16(rs, ob + 8 * 2048);
          if (__builtin_amdgcn_ballot_w64(((off2(ml[0].y, ml[1].y, ta) | off2(ml[0].w, ml[1].w, tb)) != 0u) | (umax(mx4(v[0]), mx4(v[1])) == EMPTY)) == 0ull) break;
          __builtin_amdgcn_s_sleep(1);
        }
        const float m0 = asf(ml[0].x), m1 = asf(ml[1].x);
        const float M = mx32(mx16(mx8(max3(m0, m1, m1))));
        const float e0 = __builtin_amdgcn_exp2f((m0 - M) * LOG2E), e1 = __builtin_amdgcn_exp2f((m1 - M) * LOG2E);
        float Ls = e0 * asf(ml[0].z) + e1 * asf(ml[1].z);
        Ls += dpp<ROR8>(Ls);
        Ls = sw16(Ls, Ls); Ls = sw32(Ls, Ls);
        float o[8];
#pragma unroll
        for (int j = 0; j < 4; ++j) {
          o[2 * j] = __builtin_fmaf(e0, bflo(v[0][j]), e1 * bflo(v[1][j]));
          o[2 * j + 1] = __builtin_fmaf(e0, bfhi(v[0][j]), e1 * bfhi(v[1][j]));
        }
        float t0 = sw16(sw32(o[0], o[4]), sw32(o[2], o[6])), t1 = sw16(sw32(o[1], o[5]), sw32(o[3], o[7]));
        t0 += dpp<ROR8>(t0); t1 += dpp<ROR8>(t1);
        const float inv = __builtin_amdgcn_rcpf(Ls);
        if ((lane & 8) == 0) *(LAS u32*)xw = pk2(t0 * inv, t1 * inv);
      }
      bar();
      v4u xv[4];
      load_x<4>(xr, xv);
      const f4 a0 = mma<4>(wa, xv), a1 = mma<4>(wb, xv);
      const bool up = (l15 & 2) != 0;
      const f4 a = {up ? a1.x : a0.x, up ? a1.y : a0.y, up ? a1.z : a0.z, up ? a1.w : a0.w};
      if (l15 < 4) st16(rs, o_p4, f2u(a, tag));
    }
    if (y >= 112) stage_r(16 + y - 112);
    if (cw >= 4) {
      v4u xv[4], w[4];
      load_x<4>(cf_lane + (y >> 4) * 256, xv);
      lds_wait_ge(L, C_FULL + F_GB, 1u);
      load_w<128, 4>(L + L_GT + ga0, L + L_GT + ga1, w);
      const f4 a = mma<4>(w, xv);
      if (pl) st16(rs, o_g + 3 * 64 * 8 * 128, f2u(a, tag));
    }
    if (y < 112) {
      const int fs = y >> 2;
      const int blk = 2 * (cw & 3) + (l15 >> 3), b = (l15 >> 2) & 1, fl = 16 * blk + 4 * (l15 & 3) + g4;
      float b1v = bflo(((const GAS u16*)p.b1)[128 * fs + fl]);
      int off[1] = {WS_P6 + (fs * 4 + g4) * 1024 + blk * 128 + (l15 & 7) * 16};
      lds_t tile = L + (cw >> 2) * 32768;
      lds_t pa0 = tile + ta0, pa1 = tile + ta1, pb0 = tile + tb0, pb1 = tile + tb1;
      lds_t xw = L + L_XF + 4 * XF_P + b * XF_P + fl * 2, xr = L + L_XF + 4 * XF_P + xfl;
      PIN(b1v); PIN(off[0]); PIN(pa0); PIN(pa1); PIN(pb0); PIN(pb1); PIN(xw); PIN(xr); PIN(o_p7);
      bar();
      v4u wa[4], wb[4];
      load_w<256, 4>(pa0, pa1, wa);
      load_w<256, 4>(pb0, pb1, wb);
      if (cw < 4) {
        v4u v[1];
        gather16<1>(rs, off, v, tag);
        const float hv = gelu_f(rbf(rbf(rows4(u2f(v[0]))) + b1v));
        *(LAS u16*)xw = (u16)pk2(hv, hv);
      }
      bar();
      v4u xv[4];
      load_x<4>(xr, xv);
      const f4 a0 = mma<4>(wa, xv), a1 = mma<4>(wb, xv);
      if (pl) { st16(rs, o_p7, f2u(a0, tag)); st16(rs, o_p7 + 28 * 128, f2u(a1, tag)); }
    }
  }
}
}

extern "C" void kernel_launch(void* const* d_in, const int* in_sizes, int n_in, void* d_out, int out_size, void* d_ws, size_t ws_size, hipStream_t stream) {
  using namespace mk;
  Params p;
  p.x = (const u16*)d_in[0]; p.kc = (const u16*)d_in[1]; p.vc = (const u16*)d_in[2]; p.wq = (const u16*)d_in[3]; p.wo = (const u16*)d_in[4];
  p.w1 = (const u16*)d_in[6]; p.b1 = (const u16*)d_in[7]; p.w2 = (const u16*)d_in[8]; p.b2 = (const u16*)d_in[9];
  p.c = (const u16*)d_in[10]; p.wm = (const u16*)d_in[11];
  p.out = (u16*)d_out; p.ws = (unsigned char*)d_ws;
  (void)hipFuncSetAttribute((const void*)mega, hipFuncAttributeMaxDynamicSharedMemorySize, LDS_BYTES);
  hipLaunchKernelGGL(mega, dim3(NWG), dim3(NTHR), LDS_BYTES, stream, p);
}
```
